```python
import torch

def _softmax(x, axis=-1):
    return torch.softmax(x, dim=axis)
def _gelu(x):
    return torch.nn.functional.gelu(x, approximate="tanh")
def _silu(x):
    return torch.nn.functional.silu(x)
def _sigmoid(x):
    return torch.sigmoid(x)
def _layernorm(x, axis=-1, eps=1e-5):
    m = x.mean(dim=axis, keepdim=True)
    v = x.var(dim=axis, keepdim=True, unbiased=False)
    return (x - m) / torch.sqrt(v + eps)
def _scatter_add(tgt, idx, src, axis=0):
    return tgt.clone().index_add_(axis, idx, src)
def _segment_sum(src, seg, n):
    out = torch.zeros((n,) + src.shape[1:], dtype=src.dtype)
    return out.index_add_(0, seg, src)
def _pad(x, pw):
    flat = tuple(v for pair in reversed(pw) for v in pair)
    return torch.nn.functional.pad(x, flat)

kernel_name = 'standalone__gqa_softmax__near_ridge'

def setup_inputs():

    _w = (lambda a: (lambda t: t.to(torch.bfloat16) if t.is_floating_point() else t)(torch.as_tensor(a)))
    B, Hq, Hkv, Sq, Sk, D = 32, 32, 32, 192, 512, 128
    return {
        "q": _w((lambda *s: torch.randn(*s, dtype=torch.bfloat16) * 0.8)(B, Hq, Sq, D)),
        "k": _w((lambda *s: torch.randn(*s, dtype=torch.bfloat16) * 0.8)(B, Hkv, Sk, D)),
        "v": _w((lambda *s: torch.randn(*s, dtype=torch.bfloat16) * 0.05)(B, Hkv, Sk, D)),
    }

def reference(q, k, v):
    B, Hq, Sq, D = q.shape
    Hkv = k.shape[1]
    rep = Hq // Hkv
    k = (lambda x, n, axis: torch.repeat_interleave(x, n, dim=axis))(k, rep, axis=1)
    v = (lambda x, n, axis: torch.repeat_interleave(x, n, dim=axis))(v, rep, axis=1)
    scale = D ** -0.5
    scores = torch.einsum("bhqd,bhkd->bhqk", q, k) * scale
    p = _softmax(scores, axis=-1)
    return torch.einsum("bhqk,bhkd->bhqd", p, v)
```

```cpp
#include <hip/hip_runtime.h>
#include <hip/hip_bf16.h>

typedef __bf16   bf16x8 __attribute__((ext_vector_type(8)));
typedef __bf16   bf16x4 __attribute__((ext_vector_type(4)));
typedef __bf16   bf16x2 __attribute__((ext_vector_type(2)));
typedef float    f32x4  __attribute__((ext_vector_type(4)));
typedef float    f32x2  __attribute__((ext_vector_type(2)));
typedef unsigned u32x4  __attribute__((ext_vector_type(4)));
#define LAS __attribute__((address_space(3)))
#if defined(__HIP_DEVICE_COMPILE__) && !defined(ALLOW_PK)
#define NOPK __attribute__((target("no-packed-fp32-ops")))
#else
#define NOPK
#endif
#define DI __device__ __forceinline__ NOPK
#define F2U(x) __builtin_bit_cast(unsigned, (float)(x))
#define U2F(x) __builtin_bit_cast(float, (unsigned)(x))

#define HPW 2
#define LDPOL " nt"

constexpr int NHEAD = 1024, SQ = 192, SK = 512, D = 128;
constexpr int NSLOT = 5;
constexpr unsigned SLOT = 16384;
constexpr unsigned QBYTES = SQ * D * 2, KBYTES = SK * D * 2;
constexpr unsigned OOB = 0x80000000u;
constexpr float CS = 0.08838834764831845f * 1.4426950408889634f;
constexpr float THR = 8.0f;

DI unsigned cvtpk(float lo, float hi) {
  f32x2 v = {lo, hi};
  bf16x2 b = __builtin_convertvector(v, bf16x2);
  return __builtin_bit_cast(unsigned, b);
}
DI float max3f(float a, float b, float c) { return __builtin_fmaxf(__builtin_fmaxf(a, b), c); }
DI float max2f(float a, float b) { return __builtin_fmaxf(a, b); }
DI float fadd_s(float a, float b) { return a + b; }
DI float fma_s(float a, float b, float c) { return __builtin_fmaf(a, b, c); }

DI unsigned sig(unsigned a) { return ((a & 1u) << 1) ^ ((a >> 1) * 3u); }
DI float xmax4(float x) {
  auto a = __builtin_amdgcn_permlane16_swap(F2U(x), F2U(x), false, false);
  x = __builtin_fmaxf(U2F(a[0]), U2F(a[1]));
  auto b = __builtin_amdgcn_permlane32_swap(F2U(x), F2U(x), false, false);
  return __builtin_fmaxf(U2F(b[0]), U2F(b[1]));
}
DI float xsum4(float x) {
  auto a = __builtin_amdgcn_permlane16_swap(F2U(x), F2U(x), false, false);
  x = U2F(a[0]) + U2F(a[1]);
  auto b = __builtin_amdgcn_permlane32_swap(F2U(x), F2U(x), false, false);
  return U2F(b[0]) + U2F(b[1]);
}

DI void issue_slot(unsigned m0v, unsigned voff, __amdgpu_buffer_rsrc_t ra, unsigned sa, __amdgpu_buffer_rsrc_t rb, unsigned sb) {
  asm volatile(
      "s_mov_b32 m0, %0\n\t"
      "s_nop 0\n\t"
      "buffer_load_dwordx4 %1, %2, %3 offen" LDPOL " lds\n\t"
      "buffer_load_dwordx4 %1, %2, %3 offen offset:1024" LDPOL " lds\n\t"
      "s_add_u32 m0, m0, 0x2000\n\t"
      "s_nop 0\n\t"
      "buffer_load_dwordx4 %1, %4, %5 offen" LDPOL " lds\n\t"
      "buffer_load_dwordx4 %1, %4, %5 offen offset:1024" LDPOL " lds"
      :: "s"(m0v), "v"(voff), "s"(ra), "s"(sa), "s"(rb), "s"(sb) : "memory", "scc");
}
#define WAIT_BAR(N) asm volatile("s_waitcnt vmcnt(" #N ") lgkmcnt(0)\n\ts_barrier" ::: "memory")

__global__ __launch_bounds__(256, 2) NOPK
void attn_fwd(const __bf16* __restrict__ Q, const __bf16* __restrict__ K, const __bf16* __restrict__ V, __bf16* __restrict__ O) {
  extern __shared__ __attribute__((aligned(16))) char lds[];
  const unsigned tid = __builtin_amdgcn_workitem_id_x(), lane = tid & 63u;
  const unsigned w = __builtin_amdgcn_readfirstlane(tid >> 6);
  const unsigned i16 = lane & 15u, g = lane >> 4;
  const unsigned ldsb = (unsigned)(size_t)(LAS char*)lds;

  const __amdgpu_buffer_rsrc_t rQ = __builtin_amdgcn_make_buffer_rsrc((void*)Q, (short)0, (int)(NHEAD * QBYTES), 0x00020000);
  const __amdgpu_buffer_rsrc_t rK = __builtin_amdgcn_make_buffer_rsrc((void*)K, (short)0, (int)(NHEAD * KBYTES), 0x00020000);
  const __amdgpu_buffer_rsrc_t rV = __builtin_amdgcn_make_buffer_rsrc((void*)V, (short)0, (int)(NHEAD * KBYTES), 0x00020000);

  const unsigned voff = ((lane >> 2) & 3u) * 256u + (lane >> 4) * 64u + (((lane & 3u) ^ sig(w)) << 4);
  const unsigned m0w = ldsb + w * 2048u;
  const unsigned sw = w * 2048u;
  const unsigned kbase = ldsb + (i16 >> 2) * 2048u + (i16 & 3u) * 64u + ((g ^ sig(i16 >> 2)) << 4);
  const unsigned vq = (lane >> 2) & 3u, vp = lane & 3u;
  const unsigned vbase0 = ldsb + 8192u + g * 2048u + vq * 64u + (((vp >> 1) ^ sig(g)) << 4) + 8u * (vp & 1u);

  const unsigned h0 = __builtin_amdgcn_workgroup_id_x();
  unsigned slot = 0;
  unsigned fill = 4 * SLOT;
#define adv() do { fill = slot; slot = (slot == (NSLOT - 1) * SLOT) ? 0u : slot + SLOT; } while (0)

  {
    const unsigned qo = h0 * QBYTES + sw, ko = h0 * KBYTES + sw;
    issue_slot(m0w + 0 * SLOT, voff, rQ, qo, rQ, qo + 8192u);
    issue_slot(m0w + 1 * SLOT, voff, rQ, qo + 16384u, rQ, qo + 16384u + 8192u);
    issue_slot(m0w + 2 * SLOT, voff, rQ, qo + 32768u, rQ, qo + 32768u + 8192u);
    issue_slot(m0w + 3 * SLOT, voff, rK, ko, rV, ko);
  }

  for (int hi = 0; hi < HPW; ++hi) {
    const unsigned h = h0 + hi * (NHEAD / HPW);
    const unsigned ko = h * KBYTES + sw;
    const bool more = hi + 1 < HPW;
    const unsigned qn = more ? (h + NHEAD / HPW) * QBYTES + sw : OOB;
    const unsigned kn = more ? (h + NHEAD / HPW) * KBYTES + sw : OOB;

    bf16x8 qf[3][4];
#pragma unroll
    for (int j = 0; j < 3; ++j) {
      if (hi == 0) WAIT_BAR(12); else WAIT_BAR(24);
      issue_slot(m0w + fill, voff, rK, ko + (j + 1) * 8192u, rV, ko + (j + 1) * 8192u);
      const unsigned qa = kbase + slot + (w >> 1) * 8192u + (w & 1u) * 1024u;
#pragma unroll
      for (int ks = 0; ks < 4; ++ks) qf[j][ks] = *(const LAS bf16x8*)(qa + ks * 256);
      adv();
    }

    f32x4 o[3][8];
    float m[3], l[3];
#pragma unroll
    for (int qb = 0; qb < 3; ++qb) {
      m[qb] = 0.f; l[qb] = 0.f;
#pragma unroll
      for (int db = 0; db < 8; ++db) o[qb][db] = f32x4{0.f, 0.f, 0.f, 0.f};
    }

    for (int t = 0; t < 16; ++t) {
      if (t == 0 && hi != 0) WAIT_BAR(24); else WAIT_BAR(12);
      if (t < 12) issue_slot(m0w + fill, voff, rK, ko + (t + 4) * 8192u, rV, ko + (t + 4) * 8192u);
      else if (t < 15) issue_slot(m0w + fill, voff, rQ, qn + (t - 12) * 16384u, rQ, qn + (t - 12) * 16384u + 8192u);
      else issue_slot(m0w + fill, voff, rK, kn, rV, kn);

      const unsigned ka = kbase + slot;
      f32x4 s[3][2];
#pragma unroll
      for (int kb = 0; kb < 2; ++kb) {
#pragma unroll
        for (int ks = 0; ks < 4; ++ks) {
          const bf16x8 kf = *(const LAS bf16x8*)(ka + kb * 1024 + ks * 256);
#pragma unroll
          for (int qb = 0; qb < 3; ++qb)
            s[qb][kb] = __builtin_amdgcn_mfma_f32_16x16x32_bf16(kf, qf[qb][ks], ks == 0 ? f32x4{0.f, 0.f, 0.f, 0.f} : s[qb][kb], 0, 0, 0);
        }
      }
#pragma unroll
      for (int qb = 0; qb < 3; ++qb) {
        const float nm = -m[qb];
#pragma unroll
        for (int kb = 0; kb < 2; ++kb)
#pragma unroll
          for (int r = 0; r < 4; ++r) s[qb][kb][r] = fma_s(s[qb][kb][r], CS, nm);
      }
      float mx[3];
#pragma unroll
      for (int qb = 0; qb < 3; ++qb) {
        float a = max3f(s[qb][0][0], s[qb][0][1], s[qb][0][2]);
        float b = max3f(s[qb][0][3], s[qb][1][0], s[qb][1][1]);
        a = max3f(a, s[qb][1][2], s[qb][1][3]);
        mx[qb] = max2f(a, b);
      }
      const float mall = max3f(mx[0], mx[1], mx[2]);
      if (__builtin_expect(t == 0 || __builtin_amdgcn_ballot_w64(mall > THR) != 0, 0)) {
#pragma unroll
        for (int qb = 0; qb < 3; ++qb) {
          float dl = xmax4(mx[qb]);
          if (t != 0) dl = __builtin_fmaxf(dl, 0.f);
          const float al = (t != 0) ? __builtin_amdgcn_exp2f(-dl) : 1.f;
          m[qb] += dl; l[qb] *= al;
#pragma unroll
          for (int kb = 0; kb < 2; ++kb)
#pragma unroll
            for (int r = 0; r < 4; ++r) s[qb][kb][r] -= dl;
#pragma unroll
          for (int db = 0; db < 8; ++db) o[qb][db] *= al;
        }
      }
      bf16x8 p[3];
#pragma unroll
      for (int qb = 0; qb < 3; ++qb) {
#pragma unroll
        for (int kb = 0; kb < 2; ++kb)
#pragma unroll
          for (int r = 0; r < 4; ++r) s[qb][kb][r] = __builtin_amdgcn_exp2f(s[qb][kb][r]);
        float a = fadd_s(fadd_s(s[qb][0][0], s[qb][0][1]), fadd_s(s[qb][0][2], s[qb][0][3]));
        float b = fadd_s(fadd_s(s[qb][1][0], s[qb][1][1]), fadd_s(s[qb][1][2], s[qb][1][3]));
        l[qb] = fadd_s(l[qb], fadd_s(a, b));
        u32x4 pw = {cvtpk(s[qb][0][0], s[qb][0][1]), cvtpk(s[qb][0][2], s[qb][0][3]),
                    cvtpk(s[qb][1][0], s[qb][1][1]), cvtpk(s[qb][1][2], s[qb][1][3])};
        p[qb] = __builtin_bit_cast(bf16x8, pw);
      }
      const unsigned va0 = vbase0 + slot, va1 = va0 ^ 32u;
#pragma unroll
      for (int db = 0; db < 8; ++db) {
        const unsigned va = ((db & 1) ? va1 : va0) + (db >> 1) * 256;
        const bf16x4 lo = __builtin_amdgcn_ds_read_tr16_b64_v4bf16((LAS bf16x4*)(va));
        const bf16x4 up = __builtin_amdgcn_ds_read_tr16_b64_v4bf16((LAS bf16x4*)(va + 1024));
        const bf16x8 vf = __builtin_shufflevector(lo, up, 0, 1, 2, 3, 4, 5, 6, 7);
#pragma unroll
        for (int qb = 0; qb < 3; ++qb) o[qb][db] = __builtin_amdgcn_mfma_f32_16x16x32_bf16(vf, p[qb], o[qb][db], 0, 0, 0);
      }
      adv();
    }

    char* ob = (char*)O + (size_t)h * QBYTES +
               (32u * (w >> 1) + 8u * (i16 >> 2) + 4u * (w & 1u) + (i16 & 3u)) * 256u + 32u * (g & 1u) + 16u * (g >> 1);
#pragma unroll
    for (int qb = 0; qb < 3; ++qb) {
      const float il = __builtin_amdgcn_rcpf(xsum4(l[qb]));
#pragma unroll
      for (int tt = 0; tt < 4; ++tt) {
        const f32x4 a = o[qb][2 * tt] * il, b = o[qb][2 * tt + 1] * il;
        auto r0 = __builtin_amdgcn_permlane16_swap(cvtpk(a[0], a[1]), cvtpk(b[0], b[1]), false, false);
        auto r1 = __builtin_amdgcn_permlane16_swap(cvtpk(a[2], a[3]), cvtpk(b[2], b[3]), false, false);
        const u32x4 ov = {r0[0], r1[0], r0[1], r1[1]};
        *(u32x4*)(ob + qb * 16384 + tt * 64) = ov;
      }
    }
  }
}

extern "C" void kernel_launch(void* const* d_in, const int* in_sizes, int n_in,
                              void* d_out, int out_size, void* d_ws, size_t ws_size,
                              hipStream_t stream) {
  (void)in_sizes; (void)n_in; (void)out_size; (void)d_ws; (void)ws_size;
  (void)hipFuncSetAttribute((const void*)attn_fwd, hipFuncAttributeMaxDynamicSharedMemorySize, NSLOT * SLOT);
  hipLaunchKernelGGL(attn_fwd, dim3(NHEAD / HPW), dim3(256), NSLOT * SLOT, stream,
                     (const __bf16*)d_in[0], (const __bf16*)d_in[1], (const __bf16*)d_in[2], (__bf16*)d_out);
}
```

```cpp
#include <hip/hip_runtime.h>
#include <hip/hip_bf16.h>

typedef __bf16   bf16x8 __attribute__((ext_vector_type(8)));
typedef __bf16   bf16x4 __attribute__((ext_vector_type(4)));
typedef __bf16   bf16x2 __attribute__((ext_vector_type(2)));
typedef float    f32x4  __attribute__((ext_vector_type(4)));
typedef float    f32x2  __attribute__((ext_vector_type(2)));
typedef unsigned u32x4  __attribute__((ext_vector_type(4)));
#define LAS __attribute__((address_space(3)))
#if defined(__HIP_DEVICE_COMPILE__)
#define NOPK __attribute__((target("no-packed-fp32-ops")))
#else
#define NOPK
#endif
#define DI __device__ __forceinline__ NOPK
#define F2U(x) __builtin_bit_cast(unsigned, (float)(x))
#define U2F(x) __builtin_bit_cast(float, (unsigned)(x))

#define HPW 4
#define CONTIG 0
#define SAMEHEAD 0
#define KDIAG 0
constexpr int NHEAD = 1024, SQ = 192, SK = 512, D = 128;
constexpr int NSLOT = 5;
constexpr unsigned SLOT = 16384;
constexpr unsigned QBYTES = SQ * D * 2, KBYTES = SK * D * 2;
constexpr unsigned OOB = 0x80000000u;
constexpr float CS = 0.08838834764831845f * 1.4426950408889634f;

DI unsigned cvtpk(float lo, float hi) {
  f32x2 v = {lo, hi};
  bf16x2 b = __builtin_convertvector(v, bf16x2);
  return __builtin_bit_cast(unsigned, b);
}
DI unsigned sig(unsigned a) { return ((a & 1u) << 1) ^ ((a >> 1) * 3u); }
DI float xmax4(float x) {
  auto a = __builtin_amdgcn_permlane16_swap(F2U(x), F2U(x), false, false);
  x = __builtin_fmaxf(U2F(a[0]), U2F(a[1]));
  auto b = __builtin_amdgcn_permlane32_swap(F2U(x), F2U(x), false, false);
  return __builtin_fmaxf(U2F(b[0]), U2F(b[1]));
}
DI float xsum4(float x) {
  auto a = __builtin_amdgcn_permlane16_swap(F2U(x), F2U(x), false, false);
  x = U2F(a[0]) + U2F(a[1]);
  auto b = __builtin_amdgcn_permlane32_swap(F2U(x), F2U(x), false, false);
  return U2F(b[0]) + U2F(b[1]);
}
DI void issue_slot(unsigned m0v, unsigned voff, __amdgpu_buffer_rsrc_t ra, unsigned sa, __amdgpu_buffer_rsrc_t rb, unsigned sb) {
  asm volatile(
      "s_mov_b32 m0, %0\n\t"
      "s_nop 4\n\t"
      "buffer_load_dwordx4 %1, %2, %3 offen lds\n\t"
      "buffer_load_dwordx4 %1, %2, %3 offen offset:1024 lds\n\t"
      "s_add_u32 m0, m0, 0x2000\n\t"
      "s_nop 0\n\t"
      "buffer_load_dwordx4 %1, %4, %5 offen lds\n\t"
      "buffer_load_dwordx4 %1, %4, %5 offen offset:1024 lds"
      :: "s"(m0v), "v"(voff), "s"(ra), "s"(sa), "s"(rb), "s"(sb) : "memory", "scc");
}
#define WAIT_BAR(N) asm volatile("s_waitcnt vmcnt(" #N ") lgkmcnt(0)\n\ts_barrier" ::: "memory")

DI unsigned f_voff(unsigned lane, unsigned w) { return ((lane >> 2) & 3u) * 256u + (lane >> 4) * 64u + (((lane & 3u) ^ sig(w)) << 4); }
DI unsigned f_kbase(unsigned lane) { const unsigned i16 = lane & 15u, g = lane >> 4; return (i16 >> 2) * 2048u + (i16 & 3u) * 64u + ((g ^ sig(i16 >> 2)) << 4); }
DI unsigned f_vbase(unsigned lane) { const unsigned g = lane >> 4, vq = (lane >> 2) & 3u, vp = lane & 3u;
  return 8192u + g * 2048u + vq * 64u + (((vp >> 1) ^ sig(g)) << 4) + 8u * (vp & 1u); }
DI unsigned f_obase(unsigned lane, unsigned w) { const unsigned i16 = lane & 15u, g = lane >> 4;
  return (32u * (w >> 1) + 8u * (i16 >> 2) + 4u * (w & 1u) + (i16 & 3u)) * 256u + 32u * (g & 1u) + 16u * (g >> 1); }

DI void safe_head(const __bf16* Q, const __bf16* K, const __bf16* V, __bf16* O, unsigned h, unsigned w, unsigned lane, unsigned ldsb) {
  const __amdgpu_buffer_rsrc_t rQ = __builtin_amdgcn_make_buffer_rsrc((void*)Q, (short)0, (int)(NHEAD * QBYTES), 0x00020000);
  const __amdgpu_buffer_rsrc_t rK = __builtin_amdgcn_make_buffer_rsrc((void*)K, (short)0, (int)(NHEAD * KBYTES), 0x00020000);
  const __amdgpu_buffer_rsrc_t rV = __builtin_amdgcn_make_buffer_rsrc((void*)V, (short)0, (int)(NHEAD * KBYTES), 0x00020000);
  const __amdgpu_buffer_rsrc_t rO = __builtin_amdgcn_make_buffer_rsrc((void*)O, (short)0, (int)(NHEAD * QBYTES), 0x00020000);
  const unsigned voff = f_voff(lane, w), m0w = ldsb + w * 2048u, sw = w * 2048u;
  const unsigned kbase = ldsb + f_kbase(lane), vbase0 = ldsb + f_vbase(lane);
  const unsigned qo = h * QBYTES + sw, ko = h * KBYTES + sw;
  unsigned slot = 0, fill = 4 * SLOT;
#define adv() do { fill = slot; slot = (slot == (NSLOT - 1) * SLOT) ? 0u : slot + SLOT; } while (0)
  WAIT_BAR(0);
  issue_slot(m0w + 0 * SLOT, voff, rQ, qo, rQ, qo + 8192u);
  issue_slot(m0w + 1 * SLOT, voff, rQ, qo + 16384u, rQ, qo + 16384u + 8192u);
  issue_slot(m0w + 2 * SLOT, voff, rQ, qo + 32768u, rQ, qo + 32768u + 8192u);
  issue_slot(m0w + 3 * SLOT, voff, rK, ko, rV, ko);
  bf16x8 qf[3][4];
#pragma unroll
  for (int j = 0; j < 3; ++j) {
    WAIT_BAR(12);
    issue_slot(m0w + fill, voff, rK, ko + (j + 1) * 8192u, rV, ko + (j + 1) * 8192u);
    const unsigned qa = kbase + slot + (w >> 1) * 8192u + (w & 1u) * 1024u;
#pragma unroll
    for (int ks = 0; ks < 4; ++ks) qf[j][ks] = *(const LAS bf16x8*)(size_t)(qa + ks * 256);
    adv();
  }
  f32x4 o[3][8];
  float m[3], l[3];
#pragma unroll
  for (int qb = 0; qb < 3; ++qb) {
    m[qb] = -__builtin_inff(); l[qb] = 0.f;
#pragma unroll
    for (int db = 0; db < 8; ++db) o[qb][db] = f32x4{0.f, 0.f, 0.f, 0.f};
  }
#pragma nounroll
  for (int t = 0; t < 16; ++t) {
    WAIT_BAR(12);
    const unsigned kn = t < 12 ? ko + (t + 4) * 8192u : OOB;
    issue_slot(m0w + fill, voff, rK, kn, rV, kn);
    const unsigned ka = kbase + slot;
    f32x4 s[3][2];
#pragma unroll
    for (int kb = 0; kb < 2; ++kb)
#pragma unroll
      for (int ks = 0; ks < 4; ++ks) {
        const bf16x8 kf = *(const LAS bf16x8*)(size_t)(ka + kb * 1024 + ks * 256);
#pragma unroll
        for (int qb = 0; qb < 3; ++qb)
          s[qb][kb] = __builtin_amdgcn_mfma_f32_16x16x32_bf16(kf, qf[qb][ks], ks == 0 ? f32x4{0.f, 0.f, 0.f, 0.f} : s[qb][kb], 0, 0, 0);
      }
    bf16x8 p[3];
#pragma unroll
    for (int qb = 0; qb < 3; ++qb) {
      float mx = s[qb][0][0];
#pragma unroll
      for (int e = 1; e < 8; ++e) mx = __builtin_fmaxf(mx, s[qb][e >> 2][e & 3]);
      const float mn = __builtin_fmaxf(m[qb], xmax4(mx));
      const float al = __builtin_amdgcn_exp2f((m[qb] - mn) * CS);
      m[qb] = mn;
      float ps = 0.f;
#pragma unroll
      for (int e = 0; e < 8; ++e) { const float x = __builtin_amdgcn_exp2f((s[qb][e >> 2][e & 3] - mn) * CS); s[qb][e >> 2][e & 3] = x; ps += x; }
      l[qb] = l[qb] * al + ps;
#pragma unroll
      for (int db = 0; db < 8; ++db) o[qb][db] *= al;
      const u32x4 pw = {cvtpk(s[qb][0][0], s[qb][0][1]), cvtpk(s[qb][0][2], s[qb][0][3]),
                        cvtpk(s[qb][1][0], s[qb][1][1]), cvtpk(s[qb][1][2], s[qb][1][3])};
      p[qb] = __builtin_bit_cast(bf16x8, pw);
    }
    const unsigned va0 = vbase0 + slot, va1 = va0 ^ 32u;
#pragma unroll
    for (int db = 0; db < 8; ++db) {
      const unsigned va = ((db & 1) ? va1 : va0) + (db >> 1) * 256;
      const bf16x4 lo = __builtin_amdgcn_ds_read_tr16_b64_v4bf16((LAS bf16x4*)(size_t)(va));
      const bf16x4 up = __builtin_amdgcn_ds_read_tr16_b64_v4bf16((LAS bf16x4*)(size_t)(va + 1024));
      const bf16x8 vf = __builtin_shufflevector(lo, up, 0, 1, 2, 3, 4, 5, 6, 7);
#pragma unroll
      for (int qb = 0; qb < 3; ++qb) o[qb][db] = __builtin_amdgcn_mfma_f32_16x16x32_bf16(vf, p[qb], o[qb][db], 0, 0, 0);
    }
    adv();
  }
#undef adv
  const unsigned ob = f_obase(lane, w) + h * QBYTES;
#pragma unroll
  for (int qb = 0; qb < 3; ++qb) {
    const float il = __builtin_amdgcn_rcpf(xsum4(l[qb]));
#pragma unroll
    for (int tt = 0; tt < 4; ++tt) {
      const f32x4 a = o[qb][2 * tt] * il, b = o[qb][2 * tt + 1] * il;
      auto r0 = __builtin_amdgcn_permlane16_swap(cvtpk(a[0], a[1]), cvtpk(b[0], b[1]), false, false);
      auto r1 = __builtin_amdgcn_permlane16_swap(cvtpk(a[2], a[3]), cvtpk(b[2], b[3]), false, false);
      const u32x4 ov = {r0[0], r1[0], r0[1], r1[1]};
      __builtin_amdgcn_raw_buffer_store_b128(ov, rO, ob + qb * 16384 + tt * 64, 0, 0);
    }
  }
}

constexpr unsigned QST = 65536, OST = 114688, LDS_BYTES = 163840;

__global__ __launch_bounds__(512, 2) NOPK
void attn_fwd(const __bf16* __restrict__ Q, const __bf16* __restrict__ K, const __bf16* __restrict__ V, __bf16* __restrict__ O
              ) {
  extern __shared__ __attribute__((aligned(16))) char lds[];
  const unsigned tid = __builtin_amdgcn_workitem_id_x();
  const unsigned w = __builtin_amdgcn_readfirstlane(tid >> 6);
  const unsigned ldsb = (unsigned)(size_t)(LAS char*)lds;
  const unsigned bid = SAMEHEAD ? (__builtin_amdgcn_workgroup_id_x() & 7) : __builtin_amdgcn_workgroup_id_x();
  const unsigned h0 = CONTIG ? bid * HPW : (0 ? (bid & 7u) * 128u + (bid >> 3) : bid), hs = CONTIG ? 1 : (0 ? 32 : NHEAD / HPW);
  if (w >= 4) {
    const unsigned lane = tid & 63u, hw = w - 4u;
    const unsigned long long am = 0xffffffffffffull;
    const unsigned or0 = 1 ? ldsb + OST + hw * 1024u + (lane >> 4) * 256u + (((lane & 15u) ^ (lane >> 4) ^ ((hw & 1u) << 2)) << 4)
                                  : ldsb + OST + hw * 12288u + (lane >> 4) * 256u + (((lane & 15u) ^ (lane >> 4)) << 4);
    asm volatile(
      "s_mov_b64 s[40:41], %5\n"
      "s_mov_b32 s42, 0x8000000\n"
      "s_mov_b32 s43, 0x20000\n"
      "s_mov_b64 s[44:45], %6\n"
      "s_mov_b32 s46, 0x8000000\n"
      "s_mov_b32 s47, 0x20000\n"
      "s_memrealtime s[62:63]\n"
      "s_add_u32 m0, %8, 0\n"
      "s_add_u32 s52, %14, 0\n"
      "s_and_b32 s52, s52, 0x1ffff\n"
      "s_add_u32 s52, s52, %10\n"
      "buffer_load_dwordx4 %0, s[40:43], s52 offen nt lds\n"
      "buffer_load_dwordx4 %0, s[40:43], s52 offen offset:1024 nt lds\n"
      "s_barrier\n"
      "s_add_u32 m0, %8, 16384\n"
      "s_add_u32 s52, %14, 8192\n"
      "s_and_b32 s52, s52, 0x1ffff\n"
      "s_add_u32 s52, s52, %10\n"
      "buffer_load_dwordx4 %0, s[40:43], s52 offen nt lds\n"
      "buffer_load_dwordx4 %0, s[40:43], s52 offen offset:1024 nt lds\n"
      "s_add_u32 m0, %8, 24576\n"
      "s_add_u32 s53, %14, 0\n"
      "s_and_b32 s53, s53, 0x1ffff\n"
      "s_add_u32 s53, s53, %10\n"
      "buffer_load_dwordx4 %0, s[44:47], s53 offen nt lds\n"
      "buffer_load_dwordx4 %0, s[44:47], s53 offen offset:1024 nt lds\n"
      "s_getpc_b64 s[36:37]\n"
      "HPC%=:\n"
      "s_and_b32 s37, s37, 0xffff\n"
      "s_mov_b32 s38, HEND%=-HPC%=\n"
      "s_mov_b32 s39, 0x20000\n"
      "v_mbcnt_lo_u32_b32 v0, -1, 0\n"
      "v_mbcnt_hi_u32_b32 v0, -1, v0\n"
      "v_lshlrev_b32_e32 v0, 7, v0\n"
      "buffer_load_dword v0, v0, s[36:39], %12 offen\n"
      "s_mov_b64 s[36:37], %4\n"
      "s_mov_b32 s38, 0x3000000\n"
      "s_mov_b32 s39, 0x20000\n"
      "s_mov_b64 s[48:49], %7\n"
      "s_mov_b32 s50, 0x3000000\n"
      "s_mov_b32 s51, 0x20000\n"
      "s_mov_b32 s56, 0x3e0293ee\n"
      "s_mov_b32 s57, 0xffff0000\n"
      "v_mbcnt_lo_u32_b32 v4, -1, 0\n"
      "v_mbcnt_hi_u32_b32 v4, -1, v4\n"
      "v_lshlrev_b32_e32 v4, 4, v4\n"
      "s_waitcnt vmcnt(5) lgkmcnt(0)\n"
      "s_barrier\n"
      "s_add_u32 s62, s62, %13\n"
      "s_add_u32 m0, %8, 32768\n"
      "s_add_u32 s52, %14, 16384\n"
      "s_and_b32 s52, s52, 0x1ffff\n"
      "s_add_u32 s52, s52, %10\n"
      "buffer_load_dwordx4 %0, s[40:43], s52 offen nt lds\n"
      "buffer_load_dwordx4 %0, s[40:43], s52 offen offset:1024 nt lds\n"
      "s_add_u32 m0, %8, 40960\n"
      "s_add_u32 s53, %14, 8192\n"
      "s_and_b32 s53, s53, 0x1ffff\n"
      "s_add_u32 s53, s53, %10\n"
      "buffer_load_dwordx4 %0, s[44:47], s53 offen nt lds\n"
      "buffer_load_dwordx4 %0, s[44:47], s53 offen offset:1024 nt lds\n"
      "s_add_u32 m0, %8, 49152\n"
      "s_add_u32 s52, %14, 24576\n"
      "s_and_b32 s52, s52, 0x1ffff\n"
      "s_add_u32 s52, s52, %10\n"
      "buffer_load_dwordx4 %0, s[40:43], s52 offen nt lds\n"
      "buffer_load_dwordx4 %0, s[40:43], s52 offen offset:1024 nt lds\n"
      "s_add_u32 s54, %9, 12582912\n"
      "buffer_load_dwordx4 v[8:11], %0, s[36:39], s54 offen nt\n"
      "s_add_u32 s54, %9, 12587008\n"
      "buffer_load_dwordx4 v[12:15], %0, s[36:39], s54 offen nt\n"
      "s_add_u32 s54, %9, 12591104\n"
      "buffer_load_dwordx4 v[16:19], %0, s[36:39], s54 offen nt\n"
      "s_add_u32 m0, %8, 57344\n"
      "s_add_u32 s53, %14, 16384\n"
      "s_and_b32 s53, s53, 0x1ffff\n"
      "s_add_u32 s53, s53, %10\n"
      "buffer_load_dwordx4 %0, s[44:47], s53 offen nt lds\n"
      "buffer_load_dwordx4 %0, s[44:47], s53 offen offset:1024 nt lds\n"
      "HP1_%=:\n"
      "s_memrealtime s[60:61]\n"
      "s_waitcnt lgkmcnt(0)\n"
      "s_sub_u32 s61, s60, s62\n"
      "s_cmp_lt_u32 s61, 300\n"
      "s_cbranch_scc1 HP1_%=\n"
      "s_waitcnt vmcnt(12) lgkmcnt(0)\n"
      "s_barrier\n"
      "s_memrealtime s[58:59]\n"
      "s_add_u32 m0, %8, 0\n"
      "s_add_u32 s52, %14, 32768\n"
      "s_and_b32 s52, s52, 0x1ffff\n"
      "s_add_u32 s52, s52, %10\n"
      "buffer_load_dwordx4 %0, s[40:43], s52 offen nt lds\n"
      "buffer_load_dwordx4 %0, s[40:43], s52 offen offset:1024 nt lds\n"
      "s_add_u32 s54, %9, 12595200\n"
      "buffer_load_dwordx4 v[20:23], %0, s[36:39], s54 offen nt\n"
      "s_add_u32 s54, %9, 12599296\n"
      "buffer_load_dwordx4 v[24:27], %0, s[36:39], s54 offen nt\n"
      "s_add_u32 m0, %8, 8192\n"
      "s_add_u32 s53, %14, 24576\n"
      "s_and_b32 s53, s53, 0x1ffff\n"
      "s_add_u32 s53, s53, %10\n"
      "buffer_load_dwordx4 %0, s[44:47], s53 offen nt lds\n"
      "buffer_load_dwordx4 %0, s[44:47], s53 offen offset:1024 nt lds\n"
      "HP2_%=:\n"
      "s_memrealtime s[60:61]\n"
      "s_waitcnt lgkmcnt(0)\n"
      "s_sub_u32 s61, s60, s58\n"
      "s_cmp_lt_u32 s61, 80\n"
      "s_cbranch_scc1 HP2_%=\n"
      "s_sub_u32 s61, s60, s62\n"
      "s_cmp_lt_u32 s61, 395\n"
      "s_cbranch_scc1 HP2_%=\n"
      "s_waitcnt vmcnt(13) lgkmcnt(0)\n"
      "s_barrier\n"
      "s_memrealtime s[58:59]\n"
      "s_add_u32 m0, %8, 16384\n"
      "s_add_u32 s52, %14, 40960\n"
      "s_and_b32 s52, s52, 0x1ffff\n"
      "s_add_u32 s52, s52, %10\n"
      "buffer_load_dwordx4 %0, s[40:43], s52 offen nt lds\n"
      "buffer_load_dwordx4 %0, s[40:43], s52 offen offset:1024 nt lds\n"
      "s_add_u32 s54, %9, 12603392\n"
      "buffer_load_dwordx4 v[28:31], %0, s[36:39], s54 offen nt\n"
      "s_add_u32 s54, %9, 12607488\n"
      "buffer_load_dwordx4 v[32:35], %0, s[36:39], s54 offen nt\n"
      "s_add_u32 m0, %8, 24576\n"
      "s_add_u32 s53, %14, 32768\n"
      "s_and_b32 s53, s53, 0x1ffff\n"
      "s_add_u32 s53, s53, %10\n"
      "buffer_load_dwordx4 %0, s[44:47], s53 offen nt lds\n"
      "buffer_load_dwordx4 %0, s[44:47], s53 offen offset:1024 nt lds\n"
      "HP3_%=:\n"
      "s_memrealtime s[60:61]\n"
      "s_waitcnt lgkmcnt(0)\n"
      "s_sub_u32 s61, s60, s58\n"
      "s_cmp_lt_u32 s61, 74\n"
      "s_cbranch_scc1 HP3_%=\n"
      "s_sub_u32 s61, s60, s62\n"
      "s_cmp_lt_u32 s61, 484\n"
      "s_cbranch_scc1 HP3_%=\n"
      "s_waitcnt vmcnt(12) lgkmcnt(0)\n"
      "s_barrier\n"
      "s_memrealtime s[58:59]\n"
      "s_add_u32 m0, %8, 32768\n"
      "s_add_u32 s52, %14, 49152\n"
      "s_and_b32 s52, s52, 0x1ffff\n"
      "s_add_u32 s52, s52, %10\n"
      "buffer_load_dwordx4 %0, s[40:43], s52 offen nt lds\n"
      "buffer_load_dwordx4 %0, s[40:43], s52 offen offset:1024 nt lds\n"
      "s_add_u32 s54, %9, 12611584\n"
      "buffer_load_dwordx4 v[36:39], %0, s[36:39], s54 offen nt\n"
      "s_add_u32 s54, %9, 12615680\n"
      "buffer_load_dwordx4 v[40:43], %0, s[36:39], s54 offen nt\n"
      "s_add_u32 m0, %8, 40960\n"
      "s_add_u32 s53, %14, 40960\n"
      "s_and_b32 s53, s53, 0x1ffff\n"
      "s_add_u32 s53, s53, %10\n"
      "buffer_load_dwordx4 %0, s[44:47], s53 offen nt lds\n"
      "buffer_load_dwordx4 %0, s[44:47], s53 offen offset:1024 nt lds\n"
      "v_lshlrev_b32_e32 v0, 16, v8\n"
      "v_lshlrev_b32_e32 v1, 16, v9\n"
      "v_lshlrev_b32_e32 v2, 16, v10\n"
      "v_lshlrev_b32_e32 v3, 16, v11\n"
      "v_and_b32_e32 v8, s57, v8\n"
      "v_and_b32_e32 v9, s57, v9\n"
      "v_and_b32_e32 v10, s57, v10\n"
      "v_and_b32_e32 v11, s57, v11\n"
      "v_mul_f32_e32 v0, s56, v0\n"
      "v_mul_f32_e32 v1, s56, v1\n"
      "v_mul_f32_e32 v2, s56, v2\n"
      "v_mul_f32_e32 v3, s56, v3\n"
      "v_mul_f32_e32 v8, s56, v8\n"
      "v_mul_f32_e32 v9, s56, v9\n"
      "v_mul_f32_e32 v10, s56, v10\n"
      "v_mul_f32_e32 v11, s56, v11\n"
      "v_cvt_pk_bf16_f32 v8, v0, v8\n"
      "v_cvt_pk_bf16_f32 v9, v1, v9\n"
      "v_cvt_pk_bf16_f32 v10, v2, v10\n"
      "v_cvt_pk_bf16_f32 v11, v3, v11\n"
      "v_lshlrev_b32_e32 v0, 16, v12\n"
      "v_lshlrev_b32_e32 v1, 16, v13\n"
      "v_lshlrev_b32_e32 v2, 16, v14\n"
      "v_lshlrev_b32_e32 v3, 16, v15\n"
      "v_and_b32_e32 v12, s57, v12\n"
      "v_and_b32_e32 v13, s57, v13\n"
      "v_and_b32_e32 v14, s57, v14\n"
      "v_and_b32_e32 v15, s57, v15\n"
      "v_mul_f32_e32 v0, s56, v0\n"
      "v_mul_f32_e32 v1, s56, v1\n"
      "v_mul_f32_e32 v2, s56, v2\n"
      "v_mul_f32_e32 v3, s56, v3\n"
      "v_mul_f32_e32 v12, s56, v12\n"
      "v_mul_f32_e32 v13, s56, v13\n"
      "v_mul_f32_e32 v14, s56, v14\n"
      "v_mul_f32_e32 v15, s56, v15\n"
      "v_cvt_pk_bf16_f32 v12, v0, v12\n"
      "v_cvt_pk_bf16_f32 v13, v1, v13\n"
      "v_cvt_pk_bf16_f32 v14, v2, v14\n"
      "v_cvt_pk_bf16_f32 v15, v3, v15\n"
      "HP4_%=:\n"
      "s_memrealtime s[60:61]\n"
      "s_waitcnt lgkmcnt(0)\n"
      "s_sub_u32 s61, s60, s58\n"
      "s_cmp_lt_u32 s61, 68\n"
      "s_cbranch_scc1 HP4_%=\n"
      "s_sub_u32 s61, s60, s62\n"
      "s_cmp_lt_u32 s61, 567\n"
      "s_cbranch_scc1 HP4_%=\n"
      "s_waitcnt vmcnt(12) lgkmcnt(0)\n"
      "s_barrier\n"
      "s_memrealtime s[58:59]\n"
      "ds_write_b128 %1, v[8:11] offset:0\n"
      "ds_write_b128 %1, v[12:15] offset:4096\n"
      "s_add_u32 m0, %8, 49152\n"
      "s_add_u32 s52, %14, 57344\n"
      "s_and_b32 s52, s52, 0x1ffff\n"
      "s_add_u32 s52, s52, %10\n"
      "buffer_load_dwordx4 %0, s[40:43], s52 offen nt lds\n"
      "buffer_load_dwordx4 %0, s[40:43], s52 offen offset:1024 nt lds\n"
      "s_add_u32 s54, %9, 12619776\n"
      "buffer_load_dwordx4 v[44:47], %0, s[36:39], s54 offen nt\n"
      "s_add_u32 m0, %8, 57344\n"
      "s_add_u32 s53, %14, 49152\n"
      "s_and_b32 s53, s53, 0x1ffff\n"
      "s_add_u32 s53, s53, %10\n"
      "buffer_load_dwordx4 %0, s[44:47], s53 offen nt lds\n"
      "buffer_load_dwordx4 %0, s[44:47], s53 offen offset:1024 nt lds\n"
      "v_lshlrev_b32_e32 v0, 16, v16\n"
      "v_lshlrev_b32_e32 v1, 16, v17\n"
      "v_lshlrev_b32_e32 v2, 16, v18\n"
      "v_lshlrev_b32_e32 v3, 16, v19\n"
      "v_and_b32_e32 v16, s57, v16\n"
      "v_and_b32_e32 v17, s57, v17\n"
      "v_and_b32_e32 v18, s57, v18\n"
      "v_and_b32_e32 v19, s57, v19\n"
      "v_mul_f32_e32 v0, s56, v0\n"
      "v_mul_f32_e32 v1, s56, v1\n"
      "v_mul_f32_e32 v2, s56, v2\n"
      "v_mul_f32_e32 v3, s56, v3\n"
      "v_mul_f32_e32 v16, s56, v16\n"
      "v_mul_f32_e32 v17, s56, v17\n"
      "v_mul_f32_e32 v18, s56, v18\n"
      "v_mul_f32_e32 v19, s56, v19\n"
      "v_cvt_pk_bf16_f32 v16, v0, v16\n"
      "v_cvt_pk_bf16_f32 v17, v1, v17\n"
      "v_cvt_pk_bf16_f32 v18, v2, v18\n"
      "v_cvt_pk_bf16_f32 v19, v3, v19\n"
      "HP5_%=:\n"
      "s_memrealtime s[60:61]\n"
      "s_waitcnt lgkmcnt(0)\n"
      "s_sub_u32 s61, s60, s58\n"
      "s_cmp_lt_u32 s61, 62\n"
      "s_cbranch_scc1 HP5_%=\n"
      "s_sub_u32 s61, s60, s62\n"
      "s_cmp_lt_u32 s61, 644\n"
      "s_cbranch_scc1 HP5_%=\n"
      "s_waitcnt vmcnt(11) lgkmcnt(0)\n"
      "s_barrier\n"
      "s_memrealtime s[58:59]\n"
      "ds_write_b128 %1, v[16:19] offset:8192\n"
      "s_add_u32 m0, %8, 0\n"
      "s_add_u32 s52, %14, 65536\n"
      "s_and_b32 s52, s52, 0x1ffff\n"
      "s_add_u32 s52, s52, %10\n"
      "buffer_load_dwordx4 %0, s[40:43], s52 offen nt lds\n"
      "buffer_load_dwordx4 %0, s[40:43], s52 offen offset:1024 nt lds\n"
      "s_add_u32 m0, %8, 8192\n"
      "s_add_u32 s53, %14, 57344\n"
      "s_and_b32 s53, s53, 0x1ffff\n"
      "s_add_u32 s53, s53, %10\n"
      "buffer_load_dwordx4 %0, s[44:47], s53 offen nt lds\n"
      "buffer_load_dwordx4 %0, s[44:47], s53 offen offset:1024 nt lds\n"
      "v_lshlrev_b32_e32 v0, 16, v20\n"
      "v_lshlrev_b32_e32 v1, 16, v21\n"
      "v_lshlrev_b32_e32 v2, 16, v22\n"
      "v_lshlrev_b32_e32 v3, 16, v23\n"
      "v_and_b32_e32 v20, s57, v20\n"
      "v_and_b32_e32 v21, s57, v21\n"
      "v_and_b32_e32 v22, s57, v22\n"
      "v_and_b32_e32 v23, s57, v23\n"
      "v_mul_f32_e32 v0, s56, v0\n"
      "v_mul_f32_e32 v1, s56, v1\n"
      "v_mul_f32_e32 v2, s56, v2\n"
      "v_mul_f32_e32 v3, s56, v3\n"
      "v_mul_f32_e32 v20, s56, v20\n"
      "v_mul_f32_e32 v21, s56, v21\n"
      "v_mul_f32_e32 v22, s56, v22\n"
      "v_mul_f32_e32 v23, s56, v23\n"
      "v_cvt_pk_bf16_f32 v20, v0, v20\n"
      "v_cvt_pk_bf16_f32 v21, v1, v21\n"
      "v_cvt_pk_bf16_f32 v22, v2, v22\n"
      "v_cvt_pk_bf16_f32 v23, v3, v23\n"
      "HP6_%=:\n"
      "s_memrealtime s[60:61]\n"
      "s_waitcnt lgkmcnt(0)\n"
      "s_sub_u32 s61, s60, s58\n"
      "s_cmp_lt_u32 s61, 56\n"
      "s_cbranch_scc1 HP6_%=\n"
      "s_sub_u32 s61, s60, s62\n"
      "s_cmp_lt_u32 s61, 715\n"
      "s_cbranch_scc1 HP6_%=\n"
      "s_waitcnt vmcnt(9) lgkmcnt(0)\n"
      "s_barrier\n"
      "s_memrealtime s[58:59]\n"
      "ds_write_b128 %1, v[20:23] offset:12288\n"
      "s_add_u32 m0, %8, 16384\n"
      "s_add_u32 s52, %14, 73728\n"
      "s_and_b32 s52, s52, 0x1ffff\n"
      "s_add_u32 s52, s52, %10\n"
      "buffer_load_dwordx4 %0, s[40:43], s52 offen nt lds\n"
      "buffer_load_dwordx4 %0, s[40:43], s52 offen offset:1024 nt lds\n"
      "s_add_u32 s54, %9, 12623872\n"
      "buffer_load_dwordx4 v[48:51], %0, s[36:39], s54 offen nt\n"
      "s_add_u32 m0, %8, 24576\n"
      "s_add_u32 s53, %14, 65536\n"
      "s_and_b32 s53, s53, 0x1ffff\n"
      "s_add_u32 s53, s53, %10\n"
      "buffer_load_dwordx4 %0, s[44:47], s53 offen nt lds\n"
      "buffer_load_dwordx4 %0, s[44:47], s53 offen offset:1024 nt lds\n"
      "v_lshlrev_b32_e32 v0, 16, v24\n"
      "v_lshlrev_b32_e32 v1, 16, v25\n"
      "v_lshlrev_b32_e32 v2, 16, v26\n"
      "v_lshlrev_b32_e32 v3, 16, v27\n"
      "v_and_b32_e32 v24, s57, v24\n"
      "v_and_b32_e32 v25, s57, v25\n"
      "v_and_b32_e32 v26, s57, v26\n"
      "v_and_b32_e32 v27, s57, v27\n"
      "v_mul_f32_e32 v0, s56, v0\n"
      "v_mul_f32_e32 v1, s56, v1\n"
      "v_mul_f32_e32 v2, s56, v2\n"
      "v_mul_f32_e32 v3, s56, v3\n"
      "v_mul_f32_e32 v24, s56, v24\n"
      "v_mul_f32_e32 v25, s56, v25\n"
      "v_mul_f32_e32 v26, s56, v26\n"
      "v_mul_f32_e32 v27, s56, v27\n"
      "v_cvt_pk_bf16_f32 v24, v0, v24\n"
      "v_cvt_pk_bf16_f32 v25, v1, v25\n"
      "v_cvt_pk_bf16_f32 v26, v2, v26\n"
      "v_cvt_pk_bf16_f32 v27, v3, v27\n"
      "HP7_%=:\n"
      "s_memrealtime s[60:61]\n"
      "s_waitcnt lgkmcnt(0)\n"
      "s_sub_u32 s61, s60, s58\n"
      "s_cmp_lt_u32 s61, 56\n"
      "s_cbranch_scc1 HP7_%=\n"
      "s_sub_u32 s61, s60, s62\n"
      "s_cmp_lt_u32 s61, 786\n"
      "s_cbranch_scc1 HP7_%=\n"
      "s_waitcnt vmcnt(9) lgkmcnt(0)\n"
      "s_barrier\n"
      "s_memrealtime s[58:59]\n"
      "ds_write_b128 %1, v[24:27] offset:16384\n"
      "s_add_u32 m0, %8, 32768\n"
      "s_add_u32 s52, %14, 81920\n"
      "s_and_b32 s52, s52, 0x1ffff\n"
      "s_add_u32 s52, s52, %10\n"
      "buffer_load_dwordx4 %0, s[40:43], s52 offen nt lds\n"
      "buffer_load_dwordx4 %0, s[40:43], s52 offen offset:1024 nt lds\n"
      "s_add_u32 s54, %9, 12627968\n"
      "buffer_load_dwordx4 v[52:55], %0, s[36:39], s54 offen nt\n"
      "s_add_u32 m0, %8, 40960\n"
      "s_add_u32 s53, %14, 73728\n"
      "s_and_b32 s53, s53, 0x1ffff\n"
      "s_add_u32 s53, s53, %10\n"
      "buffer_load_dwordx4 %0, s[44:47], s53 offen nt lds\n"
      "buffer_load_dwordx4 %0, s[44:47], s53 offen offset:1024 nt lds\n"
      "v_lshlrev_b32_e32 v0, 16, v28\n"
      "v_lshlrev_b32_e32 v1, 16, v29\n"
      "v_lshlrev_b32_e32 v2, 16, v30\n"
      "v_lshlrev_b32_e32 v3, 16, v31\n"
      "v_and_b32_e32 v28, s57, v28\n"
      "v_and_b32_e32 v29, s57, v29\n"
      "v_and_b32_e32 v30, s57, v30\n"
      "v_and_b32_e32 v31, s57, v31\n"
      "v_mul_f32_e32 v0, s56, v0\n"
      "v_mul_f32_e32 v1, s56, v1\n"
      "v_mul_f32_e32 v2, s56, v2\n"
      "v_mul_f32_e32 v3, s56, v3\n"
      "v_mul_f32_e32 v28, s56, v28\n"
      "v_mul_f32_e32 v29, s56, v29\n"
      "v_mul_f32_e32 v30, s56, v30\n"
      "v_mul_f32_e32 v31, s56, v31\n"
      "v_cvt_pk_bf16_f32 v28, v0, v28\n"
      "v_cvt_pk_bf16_f32 v29, v1, v29\n"
      "v_cvt_pk_bf16_f32 v30, v2, v30\n"
      "v_cvt_pk_bf16_f32 v31, v3, v31\n"
      "HP8_%=:\n"
      "s_memrealtime s[60:61]\n"
      "s_waitcnt lgkmcnt(0)\n"
      "s_sub_u32 s61, s60, s58\n"
      "s_cmp_lt_u32 s61, 56\n"
      "s_cbranch_scc1 HP8_%=\n"
      "s_sub_u32 s61, s60, s62\n"
      "s_cmp_lt_u32 s61, 857\n"
      "s_cbranch_scc1 HP8_%=\n"
      "s_waitcnt vmcnt(10) lgkmcnt(0)\n"
      "s_barrier\n"
      "s_memrealtime s[58:59]\n"
      "ds_write_b128 %1, v[28:31] offset:20480\n"
      "s_add_u32 m0, %8, 49152\n"
      "s_add_u32 s52, %14, 90112\n"
      "s_and_b32 s52, s52, 0x1ffff\n"
      "s_add_u32 s52, s52, %10\n"
      "buffer_load_dwordx4 %0, s[40:43], s52 offen nt lds\n"
      "buffer_load_dwordx4 %0, s[40:43], s52 offen offset:1024 nt lds\n"
      "s_add_u32 m0, %8, 57344\n"
      "s_add_u32 s53, %14, 81920\n"
      "s_and_b32 s53, s53, 0x1ffff\n"
      "s_add_u32 s53, s53, %10\n"
      "buffer_load_dwordx4 %0, s[44:47], s53 offen nt lds\n"
      "buffer_load_dwordx4 %0, s[44:47], s53 offen offset:1024 nt lds\n"
      "v_lshlrev_b32_e32 v0, 16, v32\n"
      "v_lshlrev_b32_e32 v1, 16, v33\n"
      "v_lshlrev_b32_e32 v2, 16, v34\n"
      "v_lshlrev_b32_e32 v3, 16, v35\n"
      "v_and_b32_e32 v32, s57, v32\n"
      "v_and_b32_e32 v33, s57, v33\n"
      "v_and_b32_e32 v34, s57, v34\n"
      "v_and_b32_e32 v35, s57, v35\n"
      "v_mul_f32_e32 v0, s56, v0\n"
      "v_mul_f32_e32 v1, s56, v1\n"
      "v_mul_f32_e32 v2, s56, v2\n"
      "v_mul_f32_e32 v3, s56, v3\n"
      "v_mul_f32_e32 v32, s56, v32\n"
      "v_mul_f32_e32 v33, s56, v33\n"
      "v_mul_f32_e32 v34, s56, v34\n"
      "v_mul_f32_e32 v35, s56, v35\n"
      "v_cvt_pk_bf16_f32 v32, v0, v32\n"
      "v_cvt_pk_bf16_f32 v33, v1, v33\n"
      "v_cvt_pk_bf16_f32 v34, v2, v34\n"
      "v_cvt_pk_bf16_f32 v35, v3, v35\n"
      "HP9_%=:\n"
      "s_memrealtime s[60:61]\n"
      "s_waitcnt lgkmcnt(0)\n"
      "s_sub_u32 s61, s60, s58\n"
      "s_cmp_lt_u32 s61, 56\n"
      "s_cbranch_scc1 HP9_%=\n"
      "s_sub_u32 s61, s60, s62\n"
      "s_cmp_lt_u32 s61, 928\n"
      "s_cbranch_scc1 HP9_%=\n"
      "s_waitcnt vmcnt(9) lgkmcnt(0)\n"
      "s_barrier\n"
      "s_memrealtime s[58:59]\n"
      "ds_write_b128 %1, v[32:35] offset:24576\n"
      "s_add_u32 m0, %8, 0\n"
      "s_add_u32 s52, %14, 98304\n"
      "s_and_b32 s52, s52, 0x1ffff\n"
      "s_add_u32 s52, s52, %10\n"
      "buffer_load_dwordx4 %0, s[40:43], s52 offen nt lds\n"
      "buffer_load_dwordx4 %0, s[40:43], s52 offen offset:1024 nt lds\n"
      "s_add_u32 s54, %9, 25165824\n"
      "buffer_load_dwordx4 v[56:59], %0, s[36:39], s54 offen nt\n"
      "s_add_u32 m0, %8, 8192\n"
      "s_add_u32 s53, %14, 90112\n"
      "s_and_b32 s53, s53, 0x1ffff\n"
      "s_add_u32 s53, s53, %10\n"
      "buffer_load_dwordx4 %0, s[44:47], s53 offen nt lds\n"
      "buffer_load_dwordx4 %0, s[44:47], s53 offen offset:1024 nt lds\n"
      "v_lshlrev_b32_e32 v0, 16, v36\n"
      "v_lshlrev_b32_e32 v1, 16, v37\n"
      "v_lshlrev_b32_e32 v2, 16, v38\n"
      "v_lshlrev_b32_e32 v3, 16, v39\n"
      "v_and_b32_e32 v36, s57, v36\n"
      "v_and_b32_e32 v37, s57, v37\n"
      "v_and_b32_e32 v38, s57, v38\n"
      "v_and_b32_e32 v39, s57, v39\n"
      "v_mul_f32_e32 v0, s56, v0\n"
      "v_mul_f32_e32 v1, s56, v1\n"
      "v_mul_f32_e32 v2, s56, v2\n"
      "v_mul_f32_e32 v3, s56, v3\n"
      "v_mul_f32_e32 v36, s56, v36\n"
      "v_mul_f32_e32 v37, s56, v37\n"
      "v_mul_f32_e32 v38, s56, v38\n"
      "v_mul_f32_e32 v39, s56, v39\n"
      "v_cvt_pk_bf16_f32 v36, v0, v36\n"
      "v_cvt_pk_bf16_f32 v37, v1, v37\n"
      "v_cvt_pk_bf16_f32 v38, v2, v38\n"
      "v_cvt_pk_bf16_f32 v39, v3, v39\n"
      "HP10_%=:\n"
      "s_memrealtime s[60:61]\n"
      "s_waitcnt lgkmcnt(0)\n"
      "s_sub_u32 s61, s60, s58\n"
      "s_cmp_lt_u32 s61, 56\n"
      "s_cbranch_scc1 HP10_%=\n"
      "s_sub_u32 s61, s60, s62\n"
      "s_cmp_lt_u32 s61, 999\n"
      "s_cbranch_scc1 HP10_%=\n"
      "s_waitcnt vmcnt(9) lgkmcnt(0)\n"
      "s_barrier\n"
      "s_memrealtime s[58:59]\n"
      "ds_write_b128 %1, v[36:39] offset:28672\n"
      "s_add_u32 m0, %8, 16384\n"
      "s_add_u32 s52, %14, 106496\n"
      "s_and_b32 s52, s52, 0x1ffff\n"
      "s_add_u32 s52, s52, %10\n"
      "buffer_load_dwordx4 %0, s[40:43], s52 offen nt lds\n"
      "buffer_load_dwordx4 %0, s[40:43], s52 offen offset:1024 nt lds\n"
      "s_add_u32 m0, %8, 24576\n"
      "s_add_u32 s53, %14, 98304\n"
      "s_and_b32 s53, s53, 0x1ffff\n"
      "s_add_u32 s53, s53, %10\n"
      "buffer_load_dwordx4 %0, s[44:47], s53 offen nt lds\n"
      "buffer_load_dwordx4 %0, s[44:47], s53 offen offset:1024 nt lds\n"
      "v_lshlrev_b32_e32 v0, 16, v40\n"
      "v_lshlrev_b32_e32 v1, 16, v41\n"
      "v_lshlrev_b32_e32 v2, 16, v42\n"
      "v_lshlrev_b32_e32 v3, 16, v43\n"
      "v_and_b32_e32 v40, s57, v40\n"
      "v_and_b32_e32 v41, s57, v41\n"
      "v_and_b32_e32 v42, s57, v42\n"
      "v_and_b32_e32 v43, s57, v43\n"
      "v_mul_f32_e32 v0, s56, v0\n"
      "v_mul_f32_e32 v1, s56, v1\n"
      "v_mul_f32_e32 v2, s56, v2\n"
      "v_mul_f32_e32 v3, s56, v3\n"
      "v_mul_f32_e32 v40, s56, v40\n"
      "v_mul_f32_e32 v41, s56, v41\n"
      "v_mul_f32_e32 v42, s56, v42\n"
      "v_mul_f32_e32 v43, s56, v43\n"
      "v_cvt_pk_bf16_f32 v40, v0, v40\n"
      "v_cvt_pk_bf16_f32 v41, v1, v41\n"
      "v_cvt_pk_bf16_f32 v42, v2, v42\n"
      "v_cvt_pk_bf16_f32 v43, v3, v43\n"
      "HP11_%=:\n"
      "s_memrealtime s[60:61]\n"
      "s_waitcnt lgkmcnt(0)\n"
      "s_sub_u32 s61, s60, s58\n"
      "s_cmp_lt_u32 s61, 56\n"
      "s_cbranch_scc1 HP11_%=\n"
      "s_sub_u32 s61, s60, s62\n"
      "s_cmp_lt_u32 s61, 1070\n"
      "s_cbranch_scc1 HP11_%=\n"
      "s_waitcnt vmcnt(9) lgkmcnt(0)\n"
      "s_barrier\n"
      "s_memrealtime s[58:59]\n"
      "ds_write_b128 %1, v[40:43] offset:32768\n"
      "s_add_u32 m0, %8, 32768\n"
      "s_add_u32 s52, %14, 114688\n"
      "s_and_b32 s52, s52, 0x1ffff\n"
      "s_add_u32 s52, s52, %10\n"
      "buffer_load_dwordx4 %0, s[40:43], s52 offen nt lds\n"
      "buffer_load_dwordx4 %0, s[40:43], s52 offen offset:1024 nt lds\n"
      "s_add_u32 s54, %9, 25169920\n"
      "buffer_load_dwordx4 v[60:63], %0, s[36:39], s54 offen nt\n"
      "s_add_u32 m0, %8, 40960\n"
      "s_add_u32 s53, %14, 106496\n"
      "s_and_b32 s53, s53, 0x1ffff\n"
      "s_add_u32 s53, s53, %10\n"
      "buffer_load_dwordx4 %0, s[44:47], s53 offen nt lds\n"
      "buffer_load_dwordx4 %0, s[44:47], s53 offen offset:1024 nt lds\n"
      "v_lshlrev_b32_e32 v0, 16, v44\n"
      "v_lshlrev_b32_e32 v1, 16, v45\n"
      "v_lshlrev_b32_e32 v2, 16, v46\n"
      "v_lshlrev_b32_e32 v3, 16, v47\n"
      "v_and_b32_e32 v44, s57, v44\n"
      "v_and_b32_e32 v45, s57, v45\n"
      "v_and_b32_e32 v46, s57, v46\n"
      "v_and_b32_e32 v47, s57, v47\n"
      "v_mul_f32_e32 v0, s56, v0\n"
      "v_mul_f32_e32 v1, s56, v1\n"
      "v_mul_f32_e32 v2, s56, v2\n"
      "v_mul_f32_e32 v3, s56, v3\n"
      "v_mul_f32_e32 v44, s56, v44\n"
      "v_mul_f32_e32 v45, s56, v45\n"
      "v_mul_f32_e32 v46, s56, v46\n"
      "v_mul_f32_e32 v47, s56, v47\n"
      "v_cvt_pk_bf16_f32 v44, v0, v44\n"
      "v_cvt_pk_bf16_f32 v45, v1, v45\n"
      "v_cvt_pk_bf16_f32 v46, v2, v46\n"
      "v_cvt_pk_bf16_f32 v47, v3, v47\n"
      "HP12_%=:\n"
      "s_memrealtime s[60:61]\n"
      "s_waitcnt lgkmcnt(0)\n"
      "s_sub_u32 s61, s60, s58\n"
      "s_cmp_lt_u32 s61, 56\n"
      "s_cbranch_scc1 HP12_%=\n"
      "s_sub_u32 s61, s60, s62\n"
      "s_cmp_lt_u32 s61, 1141\n"
      "s_cbranch_scc1 HP12_%=\n"
      "s_waitcnt vmcnt(9) lgkmcnt(0)\n"
      "s_barrier\n"
      "s_memrealtime s[58:59]\n"
      "ds_write_b128 %1, v[44:47] offset:36864\n"
      "s_add_u32 m0, %8, 49152\n"
      "s_add_u32 s52, %14, 122880\n"
      "s_and_b32 s52, s52, 0x1ffff\n"
      "s_add_u32 s52, s52, %10\n"
      "buffer_load_dwordx4 %0, s[40:43], s52 offen nt lds\n"
      "buffer_load_dwordx4 %0, s[40:43], s52 offen offset:1024 nt lds\n"
      "s_add_u32 s54, %9, 25174016\n"
      "buffer_load_dwordx4 v[64:67], %0, s[36:39], s54 offen nt\n"
      "s_add_u32 m0, %8, 57344\n"
      "s_add_u32 s53, %14, 114688\n"
      "s_and_b32 s53, s53, 0x1ffff\n"
      "s_add_u32 s53, s53, %10\n"
      "buffer_load_dwordx4 %0, s[44:47], s53 offen nt lds\n"
      "buffer_load_dwordx4 %0, s[44:47], s53 offen offset:1024 nt lds\n"
      "v_lshlrev_b32_e32 v0, 16, v48\n"
      "v_lshlrev_b32_e32 v1, 16, v49\n"
      "v_lshlrev_b32_e32 v2, 16, v50\n"
      "v_lshlrev_b32_e32 v3, 16, v51\n"
      "v_and_b32_e32 v48, s57, v48\n"
      "v_and_b32_e32 v49, s57, v49\n"
      "v_and_b32_e32 v50, s57, v50\n"
      "v_and_b32_e32 v51, s57, v51\n"
      "v_mul_f32_e32 v0, s56, v0\n"
      "v_mul_f32_e32 v1, s56, v1\n"
      "v_mul_f32_e32 v2, s56, v2\n"
      "v_mul_f32_e32 v3, s56, v3\n"
      "v_mul_f32_e32 v48, s56, v48\n"
      "v_mul_f32_e32 v49, s56, v49\n"
      "v_mul_f32_e32 v50, s56, v50\n"
      "v_mul_f32_e32 v51, s56, v51\n"
      "v_cvt_pk_bf16_f32 v48, v0, v48\n"
      "v_cvt_pk_bf16_f32 v49, v1, v49\n"
      "v_cvt_pk_bf16_f32 v50, v2, v50\n"
      "v_cvt_pk_bf16_f32 v51, v3, v51\n"
      "HP13_%=:\n"
      "s_memrealtime s[60:61]\n"
      "s_waitcnt lgkmcnt(0)\n"
      "s_sub_u32 s61, s60, s58\n"
      "s_cmp_lt_u32 s61, 56\n"
      "s_cbranch_scc1 HP13_%=\n"
      "s_sub_u32 s61, s60, s62\n"
      "s_cmp_lt_u32 s61, 1212\n"
      "s_cbranch_scc1 HP13_%=\n"
      "s_waitcnt vmcnt(10) lgkmcnt(0)\n"
      "s_barrier\n"
      "s_memrealtime s[58:59]\n"
      "ds_write_b128 %1, v[48:51] offset:40960\n"
      "s_add_u32 m0, %8, 0\n"
      "s_add_u32 s52, %14, 0\n"
      "s_and_b32 s52, s52, 0x1ffff\n"
      "s_add_u32 s52, s52, %10\n"
      "s_add_u32 s52, s52, 33554432\n"
      "buffer_load_dwordx4 %0, s[40:43], s52 offen nt lds\n"
      "buffer_load_dwordx4 %0, s[40:43], s52 offen offset:1024 nt lds\n"
      "s_add_u32 m0, %8, 8192\n"
      "s_add_u32 s53, %14, 122880\n"
      "s_and_b32 s53, s53, 0x1ffff\n"
      "s_add_u32 s53, s53, %10\n"
      "buffer_load_dwordx4 %0, s[44:47], s53 offen nt lds\n"
      "buffer_load_dwordx4 %0, s[44:47], s53 offen offset:1024 nt lds\n"
      "v_lshlrev_b32_e32 v0, 16, v52\n"
      "v_lshlrev_b32_e32 v1, 16, v53\n"
      "v_lshlrev_b32_e32 v2, 16, v54\n"
      "v_lshlrev_b32_e32 v3, 16, v55\n"
      "v_and_b32_e32 v52, s57, v52\n"
      "v_and_b32_e32 v53, s57, v53\n"
      "v_and_b32_e32 v54, s57, v54\n"
      "v_and_b32_e32 v55, s57, v55\n"
      "v_mul_f32_e32 v0, s56, v0\n"
      "v_mul_f32_e32 v1, s56, v1\n"
      "v_mul_f32_e32 v2, s56, v2\n"
      "v_mul_f32_e32 v3, s56, v3\n"
      "v_mul_f32_e32 v52, s56, v52\n"
      "v_mul_f32_e32 v53, s56, v53\n"
      "v_mul_f32_e32 v54, s56, v54\n"
      "v_mul_f32_e32 v55, s56, v55\n"
      "v_cvt_pk_bf16_f32 v52, v0, v52\n"
      "v_cvt_pk_bf16_f32 v53, v1, v53\n"
      "v_cvt_pk_bf16_f32 v54, v2, v54\n"
      "v_cvt_pk_bf16_f32 v55, v3, v55\n"
      "HP14_%=:\n"
      "s_memrealtime s[60:61]\n"
      "s_waitcnt lgkmcnt(0)\n"
      "s_sub_u32 s61, s60, s58\n"
      "s_cmp_lt_u32 s61, 56\n"
      "s_cbranch_scc1 HP14_%=\n"
      "s_sub_u32 s61, s60, s62\n"
      "s_cmp_lt_u32 s61, 1283\n"
      "s_cbranch_scc1 HP14_%=\n"
      "s_waitcnt vmcnt(9) lgkmcnt(0)\n"
      "s_barrier\n"
      "s_memrealtime s[58:59]\n"
      "ds_write_b128 %1, v[52:55] offset:45056\n"
      "s_add_u32 m0, %8, 16384\n"
      "s_add_u32 s52, %14, 8192\n"
      "s_and_b32 s52, s52, 0x1ffff\n"
      "s_add_u32 s52, s52, %10\n"
      "s_add_u32 s52, s52, 33554432\n"
      "buffer_load_dwordx4 %0, s[40:43], s52 offen nt lds\n"
      "buffer_load_dwordx4 %0, s[40:43], s52 offen offset:1024 nt lds\n"
      "s_add_u32 s54, %9, 25178112\n"
      "buffer_load_dwordx4 v[68:71], %0, s[36:39], s54 offen nt\n"
      "s_add_u32 m0, %8, 24576\n"
      "s_add_u32 s53, %14, 0\n"
      "s_and_b32 s53, s53, 0x1ffff\n"
      "s_add_u32 s53, s53, %10\n"
      "s_add_u32 s53, s53, 33554432\n"
      "buffer_load_dwordx4 %0, s[44:47], s53 offen nt lds\n"
      "buffer_load_dwordx4 %0, s[44:47], s53 offen offset:1024 nt lds\n"
      "v_lshlrev_b32_e32 v0, 16, v56\n"
      "v_lshlrev_b32_e32 v1, 16, v57\n"
      "v_lshlrev_b32_e32 v2, 16, v58\n"
      "v_lshlrev_b32_e32 v3, 16, v59\n"
      "v_and_b32_e32 v56, s57, v56\n"
      "v_and_b32_e32 v57, s57, v57\n"
      "v_and_b32_e32 v58, s57, v58\n"
      "v_and_b32_e32 v59, s57, v59\n"
      "v_mul_f32_e32 v0, s56, v0\n"
      "v_mul_f32_e32 v1, s56, v1\n"
      "v_mul_f32_e32 v2, s56, v2\n"
      "v_mul_f32_e32 v3, s56, v3\n"
      "v_mul_f32_e32 v56, s56, v56\n"
      "v_mul_f32_e32 v57, s56, v57\n"
      "v_mul_f32_e32 v58, s56, v58\n"
      "v_mul_f32_e32 v59, s56, v59\n"
      "v_cvt_pk_bf16_f32 v56, v0, v56\n"
      "v_cvt_pk_bf16_f32 v57, v1, v57\n"
      "v_cvt_pk_bf16_f32 v58, v2, v58\n"
      "v_cvt_pk_bf16_f32 v59, v3, v59\n"
      "HP15_%=:\n"
      "s_memrealtime s[60:61]\n"
      "s_waitcnt lgkmcnt(0)\n"
      "s_sub_u32 s61, s60, s58\n"
      "s_cmp_lt_u32 s61, 56\n"
      "s_cbranch_scc1 HP15_%=\n"
      "s_sub_u32 s61, s60, s62\n"
      "s_cmp_lt_u32 s61, 1354\n"
      "s_cbranch_scc1 HP15_%=\n"
      "s_waitcnt vmcnt(9) lgkmcnt(0)\n"
      "s_barrier\n"
      "s_memrealtime s[58:59]\n"
      "s_add_u32 m0, %8, 32768\n"
      "s_add_u32 s52, %14, 16384\n"
      "s_and_b32 s52, s52, 0x1ffff\n"
      "s_add_u32 s52, s52, %10\n"
      "s_add_u32 s52, s52, 33554432\n"
      "buffer_load_dwordx4 %0, s[40:43], s52 offen nt lds\n"
      "buffer_load_dwordx4 %0, s[40:43], s52 offen offset:1024 nt lds\n"
      "s_add_u32 m0, %8, 40960\n"
      "s_add_u32 s53, %14, 8192\n"
      "s_and_b32 s53, s53, 0x1ffff\n"
      "s_add_u32 s53, s53, %10\n"
      "s_add_u32 s53, s53, 33554432\n"
      "buffer_load_dwordx4 %0, s[44:47], s53 offen nt lds\n"
      "buffer_load_dwordx4 %0, s[44:47], s53 offen offset:1024 nt lds\n"
      "v_lshlrev_b32_e32 v0, 16, v60\n"
      "v_lshlrev_b32_e32 v1, 16, v61\n"
      "v_lshlrev_b32_e32 v2, 16, v62\n"
      "v_lshlrev_b32_e32 v3, 16, v63\n"
      "v_and_b32_e32 v60, s57, v60\n"
      "v_and_b32_e32 v61, s57, v61\n"
      "v_and_b32_e32 v62, s57, v62\n"
      "v_and_b32_e32 v63, s57, v63\n"
      "v_mul_f32_e32 v0, s56, v0\n"
      "v_mul_f32_e32 v1, s56, v1\n"
      "v_mul_f32_e32 v2, s56, v2\n"
      "v_mul_f32_e32 v3, s56, v3\n"
      "v_mul_f32_e32 v60, s56, v60\n"
      "v_mul_f32_e32 v61, s56, v61\n"
      "v_mul_f32_e32 v62, s56, v62\n"
      "v_mul_f32_e32 v63, s56, v63\n"
      "v_cvt_pk_bf16_f32 v60, v0, v60\n"
      "v_cvt_pk_bf16_f32 v61, v1, v61\n"
      "v_cvt_pk_bf16_f32 v62, v2, v62\n"
      "v_cvt_pk_bf16_f32 v63, v3, v63\n"
      "HP16_%=:\n"
      "s_memrealtime s[60:61]\n"
      "s_waitcnt lgkmcnt(0)\n"
      "s_sub_u32 s61, s60, s58\n"
      "s_cmp_lt_u32 s61, 56\n"
      "s_cbranch_scc1 HP16_%=\n"
      "s_sub_u32 s61, s60, s62\n"
      "s_cmp_lt_u32 s61, 1425\n"
      "s_cbranch_scc1 HP16_%=\n"
      "s_waitcnt vmcnt(9) lgkmcnt(0)\n"
      "s_barrier\n"
      "s_memrealtime s[58:59]\n"
      "ds_write_b128 %1, v[56:59] offset:0\n"
      "s_add_u32 m0, %8, 49152\n"
      "s_add_u32 s52, %14, 24576\n"
      "s_and_b32 s52, s52, 0x1ffff\n"
      "s_add_u32 s52, s52, %10\n"
      "s_add_u32 s52, s52, 33554432\n"
      "buffer_load_dwordx4 %0, s[40:43], s52 offen nt lds\n"
      "buffer_load_dwordx4 %0, s[40:43], s52 offen offset:1024 nt lds\n"
      "s_add_u32 s54, %9, 25182208\n"
      "buffer_load_dwordx4 v[72:75], %0, s[36:39], s54 offen nt\n"
      "s_add_u32 m0, %8, 57344\n"
      "s_add_u32 s53, %14, 16384\n"
      "s_and_b32 s53, s53, 0x1ffff\n"
      "s_add_u32 s53, s53, %10\n"
      "s_add_u32 s53, s53, 33554432\n"
      "buffer_load_dwordx4 %0, s[44:47], s53 offen nt lds\n"
      "buffer_load_dwordx4 %0, s[44:47], s53 offen offset:1024 nt lds\n"
      "v_lshlrev_b32_e32 v0, 16, v64\n"
      "v_lshlrev_b32_e32 v1, 16, v65\n"
      "v_lshlrev_b32_e32 v2, 16, v66\n"
      "v_lshlrev_b32_e32 v3, 16, v67\n"
      "v_and_b32_e32 v64, s57, v64\n"
      "v_and_b32_e32 v65, s57, v65\n"
      "v_and_b32_e32 v66, s57, v66\n"
      "v_and_b32_e32 v67, s57, v67\n"
      "v_mul_f32_e32 v0, s56, v0\n"
      "v_mul_f32_e32 v1, s56, v1\n"
      "v_mul_f32_e32 v2, s56, v2\n"
      "v_mul_f32_e32 v3, s56, v3\n"
      "v_mul_f32_e32 v64, s56, v64\n"
      "v_mul_f32_e32 v65, s56, v65\n"
      "v_mul_f32_e32 v66, s56, v66\n"
      "v_mul_f32_e32 v67, s56, v67\n"
      "v_cvt_pk_bf16_f32 v64, v0, v64\n"
      "v_cvt_pk_bf16_f32 v65, v1, v65\n"
      "v_cvt_pk_bf16_f32 v66, v2, v66\n"
      "v_cvt_pk_bf16_f32 v67, v3, v67\n"
      "HP17_%=:\n"
      "s_memrealtime s[60:61]\n"
      "s_waitcnt lgkmcnt(0)\n"
      "s_sub_u32 s61, s60, s58\n"
      "s_cmp_lt_u32 s61, 56\n"
      "s_cbranch_scc1 HP17_%=\n"
      "s_sub_u32 s61, s60, s62\n"
      "s_cmp_lt_u32 s61, 1496\n"
      "s_cbranch_scc1 HP17_%=\n"
      "s_waitcnt vmcnt(9) lgkmcnt(0)\n"
      "s_barrier\n"
      "s_memrealtime s[58:59]\n"
      "ds_write_b128 %1, v[60:63] offset:4096\n"
      "s_add_u32 m0, %8, 0\n"
      "s_add_u32 s52, %14, 32768\n"
      "s_and_b32 s52, s52, 0x1ffff\n"
      "s_add_u32 s52, s52, %10\n"
      "s_add_u32 s52, s52, 33554432\n"
      "buffer_load_dwordx4 %0, s[40:43], s52 offen nt lds\n"
      "buffer_load_dwordx4 %0, s[40:43], s52 offen offset:1024 nt lds\n"
      "s_add_u32 s54, %9, 25186304\n"
      "buffer_load_dwordx4 v[76:79], %0, s[36:39], s54 offen nt\n"
      "s_add_u32 s54, %9, 25190400\n"
      "buffer_load_dwordx4 v[80:83], %0, s[36:39], s54 offen nt\n"
      "s_add_u32 s54, %9, 25194496\n"
      "buffer_load_dwordx4 v[84:87], %0, s[36:39], s54 offen nt\n"
      "s_add_u32 m0, %8, 8192\n"
      "s_add_u32 s53, %14, 24576\n"
      "s_and_b32 s53, s53, 0x1ffff\n"
      "s_add_u32 s53, s53, %10\n"
      "s_add_u32 s53, s53, 33554432\n"
      "buffer_load_dwordx4 %0, s[44:47], s53 offen nt lds\n"
      "buffer_load_dwordx4 %0, s[44:47], s53 offen offset:1024 nt lds\n"
      "v_lshlrev_b32_e32 v0, 16, v68\n"
      "v_lshlrev_b32_e32 v1, 16, v69\n"
      "v_lshlrev_b32_e32 v2, 16, v70\n"
      "v_lshlrev_b32_e32 v3, 16, v71\n"
      "v_and_b32_e32 v68, s57, v68\n"
      "v_and_b32_e32 v69, s57, v69\n"
      "v_and_b32_e32 v70, s57, v70\n"
      "v_and_b32_e32 v71, s57, v71\n"
      "v_mul_f32_e32 v0, s56, v0\n"
      "v_mul_f32_e32 v1, s56, v1\n"
      "v_mul_f32_e32 v2, s56, v2\n"
      "v_mul_f32_e32 v3, s56, v3\n"
      "v_mul_f32_e32 v68, s56, v68\n"
      "v_mul_f32_e32 v69, s56, v69\n"
      "v_mul_f32_e32 v70, s56, v70\n"
      "v_mul_f32_e32 v71, s56, v71\n"
      "v_cvt_pk_bf16_f32 v68, v0, v68\n"
      "v_cvt_pk_bf16_f32 v69, v1, v69\n"
      "v_cvt_pk_bf16_f32 v70, v2, v70\n"
      "v_cvt_pk_bf16_f32 v71, v3, v71\n"
      "HP18_%=:\n"
      "s_memrealtime s[60:61]\n"
      "s_waitcnt lgkmcnt(0)\n"
      "s_sub_u32 s61, s60, s58\n"
      "s_cmp_lt_u32 s61, 56\n"
      "s_cbranch_scc1 HP18_%=\n"
      "s_sub_u32 s61, s60, s62\n"
      "s_cmp_lt_u32 s61, 1602\n"
      "s_cbranch_scc1 HP18_%=\n"
      "s_waitcnt vmcnt(12) lgkmcnt(0)\n"
      "s_barrier\n"
      "s_memrealtime s[58:59]\n"
      "ds_write_b128 %1, v[64:67] offset:8192\n"
      "s_add_u32 m0, %8, 16384\n"
      "s_add_u32 s52, %14, 40960\n"
      "s_and_b32 s52, s52, 0x1ffff\n"
      "s_add_u32 s52, s52, %10\n"
      "s_add_u32 s52, s52, 33554432\n"
      "buffer_load_dwordx4 %0, s[40:43], s52 offen nt lds\n"
      "buffer_load_dwordx4 %0, s[40:43], s52 offen offset:1024 nt lds\n"
      "s_add_u32 s54, %9, 25198592\n"
      "buffer_load_dwordx4 v[88:91], %0, s[36:39], s54 offen nt\n"
      "s_add_u32 m0, %8, 24576\n"
      "s_add_u32 s53, %14, 32768\n"
      "s_and_b32 s53, s53, 0x1ffff\n"
      "s_add_u32 s53, s53, %10\n"
      "s_add_u32 s53, s53, 33554432\n"
      "buffer_load_dwordx4 %0, s[44:47], s53 offen nt lds\n"
      "buffer_load_dwordx4 %0, s[44:47], s53 offen offset:1024 nt lds\n"
      "HP19_%=:\n"
      "s_memrealtime s[60:61]\n"
      "s_waitcnt lgkmcnt(0)\n"
      "s_sub_u32 s61, s60, s58\n"
      "s_cmp_lt_u32 s61, 56\n"
      "s_cbranch_scc1 HP19_%=\n"
      "s_sub_u32 s61, s60, s62\n"
      "s_cmp_lt_u32 s61, 1673\n"
      "s_cbranch_scc1 HP19_%=\n"
      "s_waitcnt vmcnt(12) lgkmcnt(0)\n"
      "s_barrier\n"
      "s_memrealtime s[58:59]\n"
      "ds_write_b128 %1, v[68:71] offset:12288\n"
      "s_add_u32 m0, %8, 32768\n"
      "s_add_u32 s52, %14, 49152\n"
      "s_and_b32 s52, s52, 0x1ffff\n"
      "s_add_u32 s52, s52, %10\n"
      "s_add_u32 s52, s52, 33554432\n"
      "buffer_load_dwordx4 %0, s[40:43], s52 offen nt lds\n"
      "buffer_load_dwordx4 %0, s[40:43], s52 offen offset:1024 nt lds\n"
      "s_add_u32 m0, %8, 40960\n"
      "s_add_u32 s53, %14, 40960\n"
      "s_and_b32 s53, s53, 0x1ffff\n"
      "s_add_u32 s53, s53, %10\n"
      "s_add_u32 s53, s53, 33554432\n"
      "buffer_load_dwordx4 %0, s[44:47], s53 offen nt lds\n"
      "buffer_load_dwordx4 %0, s[44:47], s53 offen offset:1024 nt lds\n"
      "v_lshlrev_b32_e32 v0, 16, v72\n"
      "v_lshlrev_b32_e32 v1, 16, v73\n"
      "v_lshlrev_b32_e32 v2, 16, v74\n"
      "v_lshlrev_b32_e32 v3, 16, v75\n"
      "v_and_b32_e32 v72, s57, v72\n"
      "v_and_b32_e32 v73, s57, v73\n"
      "v_and_b32_e32 v74, s57, v74\n"
      "v_and_b32_e32 v75, s57, v75\n"
      "v_mul_f32_e32 v0, s56, v0\n"
      "v_mul_f32_e32 v1, s56, v1\n"
      "v_mul_f32_e32 v2, s56, v2\n"
      "v_mul_f32_e32 v3, s56, v3\n"
      "v_mul_f32_e32 v72, s56, v72\n"
      "v_mul_f32_e32 v73, s56, v73\n"
      "v_mul_f32_e32 v74, s56, v74\n"
      "v_mul_f32_e32 v75, s56, v75\n"
      "v_cvt_pk_bf16_f32 v72, v0, v72\n"
      "v_cvt_pk_bf16_f32 v73, v1, v73\n"
      "v_cvt_pk_bf16_f32 v74, v2, v74\n"
      "v_cvt_pk_bf16_f32 v75, v3, v75\n"
      "HP20_%=:\n"
      "s_memrealtime s[60:61]\n"
      "s_waitcnt lgkmcnt(0)\n"
      "s_sub_u32 s61, s60, s58\n"
      "s_cmp_lt_u32 s61, 56\n"
      "s_cbranch_scc1 HP20_%=\n"
      "s_sub_u32 s61, s60, s62\n"
      "s_cmp_lt_u32 s61, 1744\n"
      "s_cbranch_scc1 HP20_%=\n"
      "s_waitcnt vmcnt(9) lgkmcnt(0)\n"
      "s_barrier\n"
      "s_memrealtime s[58:59]\n"
      "ds_write_b128 %1, v[72:75] offset:16384\n"
      "s_add_u32 m0, %8, 49152\n"
      "s_add_u32 s52, %14, 57344\n"
      "s_and_b32 s52, s52, 0x1ffff\n"
      "s_add_u32 s52, s52, %10\n"
      "s_add_u32 s52, s52, 33554432\n"
      "buffer_load_dwordx4 %0, s[40:43], s52 offen nt lds\n"
      "buffer_load_dwordx4 %0, s[40:43], s52 offen offset:1024 nt lds\n"
      "s_add_u32 s54, %9, 25202688\n"
      "buffer_load_dwordx4 v[92:95], %0, s[36:39], s54 offen nt\n"
      "s_add_u32 m0, %8, 57344\n"
      "s_add_u32 s53, %14, 49152\n"
      "s_and_b32 s53, s53, 0x1ffff\n"
      "s_add_u32 s53, s53, %10\n"
      "s_add_u32 s53, s53, 33554432\n"
      "buffer_load_dwordx4 %0, s[44:47], s53 offen nt lds\n"
      "buffer_load_dwordx4 %0, s[44:47], s53 offen offset:1024 nt lds\n"
      "v_lshlrev_b32_e32 v0, 16, v76\n"
      "v_lshlrev_b32_e32 v1, 16, v77\n"
      "v_lshlrev_b32_e32 v2, 16, v78\n"
      "v_lshlrev_b32_e32 v3, 16, v79\n"
      "v_and_b32_e32 v76, s57, v76\n"
      "v_and_b32_e32 v77, s57, v77\n"
      "v_and_b32_e32 v78, s57, v78\n"
      "v_and_b32_e32 v79, s57, v79\n"
      "v_mul_f32_e32 v0, s56, v0\n"
      "v_mul_f32_e32 v1, s56, v1\n"
      "v_mul_f32_e32 v2, s56, v2\n"
      "v_mul_f32_e32 v3, s56, v3\n"
      "v_mul_f32_e32 v76, s56, v76\n"
      "v_mul_f32_e32 v77, s56, v77\n"
      "v_mul_f32_e32 v78, s56, v78\n"
      "v_mul_f32_e32 v79, s56, v79\n"
      "v_cvt_pk_bf16_f32 v76, v0, v76\n"
      "v_cvt_pk_bf16_f32 v77, v1, v77\n"
      "v_cvt_pk_bf16_f32 v78, v2, v78\n"
      "v_cvt_pk_bf16_f32 v79, v3, v79\n"
      "HP21_%=:\n"
      "s_memrealtime s[60:61]\n"
      "s_waitcnt lgkmcnt(0)\n"
      "s_sub_u32 s61, s60, s58\n"
      "s_cmp_lt_u32 s61, 56\n"
      "s_cbranch_scc1 HP21_%=\n"
      "s_sub_u32 s61, s60, s62\n"
      "s_cmp_lt_u32 s61, 1815\n"
      "s_cbranch_scc1 HP21_%=\n"
      "s_waitcnt vmcnt(9) lgkmcnt(0)\n"
      "s_barrier\n"
      "s_memrealtime s[58:59]\n"
      "ds_write_b128 %1, v[76:79] offset:20480\n"
      "ds_read_b128 v[96:99], %2 offset:0\n"
      "s_add_u32 m0, %8, 0\n"
      "s_add_u32 s52, %14, 65536\n"
      "s_and_b32 s52, s52, 0x1ffff\n"
      "s_add_u32 s52, s52, %10\n"
      "s_add_u32 s52, s52, 33554432\n"
      "buffer_load_dwordx4 %0, s[40:43], s52 offen nt lds\n"
      "buffer_load_dwordx4 %0, s[40:43], s52 offen offset:1024 nt lds\n"
      "s_add_u32 s54, %9, 25206784\n"
      "buffer_load_dwordx4 v[100:103], %0, s[36:39], s54 offen nt\n"
      "s_add_u32 m0, %8, 8192\n"
      "s_add_u32 s53, %14, 57344\n"
      "s_and_b32 s53, s53, 0x1ffff\n"
      "s_add_u32 s53, s53, %10\n"
      "s_add_u32 s53, s53, 33554432\n"
      "buffer_load_dwordx4 %0, s[44:47], s53 offen nt lds\n"
      "buffer_load_dwordx4 %0, s[44:47], s53 offen offset:1024 nt lds\n"
      "v_lshlrev_b32_e32 v0, 16, v80\n"
      "v_lshlrev_b32_e32 v1, 16, v81\n"
      "v_lshlrev_b32_e32 v2, 16, v82\n"
      "v_lshlrev_b32_e32 v3, 16, v83\n"
      "v_and_b32_e32 v80, s57, v80\n"
      "v_and_b32_e32 v81, s57, v81\n"
      "v_and_b32_e32 v82, s57, v82\n"
      "v_and_b32_e32 v83, s57, v83\n"
      "v_mul_f32_e32 v0, s56, v0\n"
      "v_mul_f32_e32 v1, s56, v1\n"
      "v_mul_f32_e32 v2, s56, v2\n"
      "v_mul_f32_e32 v3, s56, v3\n"
      "v_mul_f32_e32 v80, s56, v80\n"
      "v_mul_f32_e32 v81, s56, v81\n"
      "v_mul_f32_e32 v82, s56, v82\n"
      "v_mul_f32_e32 v83, s56, v83\n"
      "v_cvt_pk_bf16_f32 v80, v0, v80\n"
      "v_cvt_pk_bf16_f32 v81, v1, v81\n"
      "v_cvt_pk_bf16_f32 v82, v2, v82\n"
      "v_cvt_pk_bf16_f32 v83, v3, v83\n"
      "HP22_%=:\n"
      "s_memrealtime s[60:61]\n"
      "s_waitcnt lgkmcnt(0)\n"
      "s_sub_u32 s61, s60, s58\n"
      "s_cmp_lt_u32 s61, 56\n"
      "s_cbranch_scc1 HP22_%=\n"
      "s_sub_u32 s61, s60, s62\n"
      "s_cmp_lt_u32 s61, 1886\n"
      "s_cbranch_scc1 HP22_%=\n"
      "s_waitcnt vmcnt(10) lgkmcnt(0)\n"
      "s_barrier\n"
      "s_memrealtime s[58:59]\n"
      "ds_write_b128 %1, v[80:83] offset:24576\n"
      "ds_read_b128 v[104:107], %2 offset:4096\n"
      "s_add_u32 m0, %8, 16384\n"
      "s_add_u32 s52, %14, 73728\n"
      "s_and_b32 s52, s52, 0x1ffff\n"
      "s_add_u32 s52, s52, %10\n"
      "s_add_u32 s52, s52, 33554432\n"
      "buffer_load_dwordx4 %0, s[40:43], s52 offen nt lds\n"
      "buffer_load_dwordx4 %0, s[40:43], s52 offen offset:1024 nt lds\n"
      "s_add_u32 m0, %8, 24576\n"
      "s_add_u32 s53, %14, 65536\n"
      "s_and_b32 s53, s53, 0x1ffff\n"
      "s_add_u32 s53, s53, %10\n"
      "s_add_u32 s53, s53, 33554432\n"
      "buffer_load_dwordx4 %0, s[44:47], s53 offen nt lds\n"
      "buffer_load_dwordx4 %0, s[44:47], s53 offen offset:1024 nt lds\n"
      "v_lshlrev_b32_e32 v0, 16, v84\n"
      "v_lshlrev_b32_e32 v1, 16, v85\n"
      "v_lshlrev_b32_e32 v2, 16, v86\n"
      "v_lshlrev_b32_e32 v3, 16, v87\n"
      "v_and_b32_e32 v84, s57, v84\n"
      "v_and_b32_e32 v85, s57, v85\n"
      "v_and_b32_e32 v86, s57, v86\n"
      "v_and_b32_e32 v87, s57, v87\n"
      "v_mul_f32_e32 v0, s56, v0\n"
      "v_mul_f32_e32 v1, s56, v1\n"
      "v_mul_f32_e32 v2, s56, v2\n"
      "v_mul_f32_e32 v3, s56, v3\n"
      "v_mul_f32_e32 v84, s56, v84\n"
      "v_mul_f32_e32 v85, s56, v85\n"
      "v_mul_f32_e32 v86, s56, v86\n"
      "v_mul_f32_e32 v87, s56, v87\n"
      "v_cvt_pk_bf16_f32 v84, v0, v84\n"
      "v_cvt_pk_bf16_f32 v85, v1, v85\n"
      "v_cvt_pk_bf16_f32 v86, v2, v86\n"
      "v_cvt_pk_bf16_f32 v87, v3, v87\n"
      "HP23_%=:\n"
      "s_memrealtime s[60:61]\n"
      "s_waitcnt lgkmcnt(0)\n"
      "s_sub_u32 s61, s60, s58\n"
      "s_cmp_lt_u32 s61, 56\n"
      "s_cbranch_scc1 HP23_%=\n"
      "s_sub_u32 s61, s60, s62\n"
      "s_cmp_lt_u32 s61, 1957\n"
      "s_cbranch_scc1 HP23_%=\n"
      "s_waitcnt vmcnt(9) lgkmcnt(0)\n"
      "s_barrier\n"
      "s_memrealtime s[58:59]\n"
      "ds_write_b128 %1, v[84:87] offset:28672\n"
      "ds_read_b128 v[108:111], %2 offset:8192\n"
      "s_add_u32 m0, %8, 32768\n"
      "s_add_u32 s52, %14, 81920\n"
      "s_and_b32 s52, s52, 0x1ffff\n"
      "s_add_u32 s52, s52, %10\n"
      "s_add_u32 s52, s52, 33554432\n"
      "buffer_load_dwordx4 %0, s[40:43], s52 offen nt lds\n"
      "buffer_load_dwordx4 %0, s[40:43], s52 offen offset:1024 nt lds\n"
      "s_add_u32 s54, %9, 25210880\n"
      "buffer_load_dwordx4 v[112:115], %0, s[36:39], s54 offen nt\n"
      "s_add_u32 m0, %8, 40960\n"
      "s_add_u32 s53, %14, 73728\n"
      "s_and_b32 s53, s53, 0x1ffff\n"
      "s_add_u32 s53, s53, %10\n"
      "s_add_u32 s53, s53, 33554432\n"
      "buffer_load_dwordx4 %0, s[44:47], s53 offen nt lds\n"
      "buffer_load_dwordx4 %0, s[44:47], s53 offen offset:1024 nt lds\n"
      "v_lshlrev_b32_e32 v0, 16, v88\n"
      "v_lshlrev_b32_e32 v1, 16, v89\n"
      "v_lshlrev_b32_e32 v2, 16, v90\n"
      "v_lshlrev_b32_e32 v3, 16, v91\n"
      "v_and_b32_e32 v88, s57, v88\n"
      "v_and_b32_e32 v89, s57, v89\n"
      "v_and_b32_e32 v90, s57, v90\n"
      "v_and_b32_e32 v91, s57, v91\n"
      "v_mul_f32_e32 v0, s56, v0\n"
      "v_mul_f32_e32 v1, s56, v1\n"
      "v_mul_f32_e32 v2, s56, v2\n"
      "v_mul_f32_e32 v3, s56, v3\n"
      "v_mul_f32_e32 v88, s56, v88\n"
      "v_mul_f32_e32 v89, s56, v89\n"
      "v_mul_f32_e32 v90, s56, v90\n"
      "v_mul_f32_e32 v91, s56, v91\n"
      "v_cvt_pk_bf16_f32 v88, v0, v88\n"
      "v_cvt_pk_bf16_f32 v89, v1, v89\n"
      "v_cvt_pk_bf16_f32 v90, v2, v90\n"
      "v_cvt_pk_bf16_f32 v91, v3, v91\n"
      "HP24_%=:\n"
      "s_memrealtime s[60:61]\n"
      "s_waitcnt lgkmcnt(0)\n"
      "s_sub_u32 s61, s60, s58\n"
      "s_cmp_lt_u32 s61, 56\n"
      "s_cbranch_scc1 HP24_%=\n"
      "s_sub_u32 s61, s60, s62\n"
      "s_cmp_lt_u32 s61, 2028\n"
      "s_cbranch_scc1 HP24_%=\n"
      "s_waitcnt vmcnt(9) lgkmcnt(0)\n"
      "s_barrier\n"
      "s_memrealtime s[58:59]\n"
      "ds_write_b128 %1, v[88:91] offset:32768\n"
      "ds_read_b128 v[116:119], %2 offset:12288\n"
      "s_add_u32 m0, %8, 49152\n"
      "s_add_u32 s52, %14, 90112\n"
      "s_and_b32 s52, s52, 0x1ffff\n"
      "s_add_u32 s52, s52, %10\n"
      "s_add_u32 s52, s52, 33554432\n"
      "buffer_load_dwordx4 %0, s[40:43], s52 offen nt lds\n"
      "buffer_load_dwordx4 %0, s[40:43], s52 offen offset:1024 nt lds\n"
      "s_add_u32 m0, %8, 57344\n"
      "s_add_u32 s53, %14, 81920\n"
      "s_and_b32 s53, s53, 0x1ffff\n"
      "s_add_u32 s53, s53, %10\n"
      "s_add_u32 s53, s53, 33554432\n"
      "buffer_load_dwordx4 %0, s[44:47], s53 offen nt lds\n"
      "buffer_load_dwordx4 %0, s[44:47], s53 offen offset:1024 nt lds\n"
      "v_lshlrev_b32_e32 v0, 16, v92\n"
      "v_lshlrev_b32_e32 v1, 16, v93\n"
      "v_lshlrev_b32_e32 v2, 16, v94\n"
      "v_lshlrev_b32_e32 v3, 16, v95\n"
      "v_and_b32_e32 v92, s57, v92\n"
      "v_and_b32_e32 v93, s57, v93\n"
      "v_and_b32_e32 v94, s57, v94\n"
      "v_and_b32_e32 v95, s57, v95\n"
      "v_mul_f32_e32 v0, s56, v0\n"
      "v_mul_f32_e32 v1, s56, v1\n"
      "v_mul_f32_e32 v2, s56, v2\n"
      "v_mul_f32_e32 v3, s56, v3\n"
      "v_mul_f32_e32 v92, s56, v92\n"
      "v_mul_f32_e32 v93, s56, v93\n"
      "v_mul_f32_e32 v94, s56, v94\n"
      "v_mul_f32_e32 v95, s56, v95\n"
      "v_cvt_pk_bf16_f32 v92, v0, v92\n"
      "v_cvt_pk_bf16_f32 v93, v1, v93\n"
      "v_cvt_pk_bf16_f32 v94, v2, v94\n"
      "v_cvt_pk_bf16_f32 v95, v3, v95\n"
      "HP25_%=:\n"
      "s_memrealtime s[60:61]\n"
      "s_waitcnt lgkmcnt(0)\n"
      "s_sub_u32 s61, s60, s58\n"
      "s_cmp_lt_u32 s61, 56\n"
      "s_cbranch_scc1 HP25_%=\n"
      "s_sub_u32 s61, s60, s62\n"
      "s_cmp_lt_u32 s61, 2099\n"
      "s_cbranch_scc1 HP25_%=\n"
      "s_waitcnt vmcnt(9) lgkmcnt(0)\n"
      "s_barrier\n"
      "s_memrealtime s[58:59]\n"
      "ds_write_b128 %1, v[92:95] offset:36864\n"
      "ds_read_b128 v[120:123], %2 offset:16384\n"
      "s_add_u32 m0, %8, 0\n"
      "s_add_u32 s52, %14, 98304\n"
      "s_and_b32 s52, s52, 0x1ffff\n"
      "s_add_u32 s52, s52, %10\n"
      "s_add_u32 s52, s52, 33554432\n"
      "buffer_load_dwordx4 %0, s[40:43], s52 offen nt lds\n"
      "buffer_load_dwordx4 %0, s[40:43], s52 offen offset:1024 nt lds\n"
      "s_add_u32 s54, %9, 37748736\n"
      "buffer_load_dwordx4 v[124:127], %0, s[36:39], s54 offen nt\n"
      "s_add_u32 m0, %8, 8192\n"
      "s_add_u32 s53, %14, 90112\n"
      "s_and_b32 s53, s53, 0x1ffff\n"
      "s_add_u32 s53, s53, %10\n"
      "s_add_u32 s53, s53, 33554432\n"
      "buffer_load_dwordx4 %0, s[44:47], s53 offen nt lds\n"
      "buffer_load_dwordx4 %0, s[44:47], s53 offen offset:1024 nt lds\n"
      "v_lshlrev_b32_e32 v0, 16, v100\n"
      "v_lshlrev_b32_e32 v1, 16, v101\n"
      "v_lshlrev_b32_e32 v2, 16, v102\n"
      "v_lshlrev_b32_e32 v3, 16, v103\n"
      "v_and_b32_e32 v100, s57, v100\n"
      "v_and_b32_e32 v101, s57, v101\n"
      "v_and_b32_e32 v102, s57, v102\n"
      "v_and_b32_e32 v103, s57, v103\n"
      "v_mul_f32_e32 v0, s56, v0\n"
      "v_mul_f32_e32 v1, s56, v1\n"
      "v_mul_f32_e32 v2, s56, v2\n"
      "v_mul_f32_e32 v3, s56, v3\n"
      "v_mul_f32_e32 v100, s56, v100\n"
      "v_mul_f32_e32 v101, s56, v101\n"
      "v_mul_f32_e32 v102, s56, v102\n"
      "v_mul_f32_e32 v103, s56, v103\n"
      "v_cvt_pk_bf16_f32 v100, v0, v100\n"
      "v_cvt_pk_bf16_f32 v101, v1, v101\n"
      "v_cvt_pk_bf16_f32 v102, v2, v102\n"
      "v_cvt_pk_bf16_f32 v103, v3, v103\n"
      "HP26_%=:\n"
      "s_memrealtime s[60:61]\n"
      "s_waitcnt lgkmcnt(0)\n"
      "s_sub_u32 s61, s60, s58\n"
      "s_cmp_lt_u32 s61, 56\n"
      "s_cbranch_scc1 HP26_%=\n"
      "s_sub_u32 s61, s60, s62\n"
      "s_cmp_lt_u32 s61, 2170\n"
      "s_cbranch_scc1 HP26_%=\n"
      "s_waitcnt vmcnt(9) lgkmcnt(0)\n"
      "s_barrier\n"
      "s_memrealtime s[58:59]\n"
      "ds_write_b128 %1, v[100:103] offset:40960\n"
      "ds_read_b128 v[128:131], %2 offset:20480\n"
      "s_add_u32 m0, %8, 16384\n"
      "s_add_u32 s52, %14, 106496\n"
      "s_and_b32 s52, s52, 0x1ffff\n"
      "s_add_u32 s52, s52, %10\n"
      "s_add_u32 s52, s52, 33554432\n"
      "buffer_load_dwordx4 %0, s[40:43], s52 offen nt lds\n"
      "buffer_load_dwordx4 %0, s[40:43], s52 offen offset:1024 nt lds\n"
      "s_add_u32 s54, %9, 37752832\n"
      "buffer_load_dwordx4 v[132:135], %0, s[36:39], s54 offen nt\n"
      "s_add_u32 m0, %8, 24576\n"
      "s_add_u32 s53, %14, 98304\n"
      "s_and_b32 s53, s53, 0x1ffff\n"
      "s_add_u32 s53, s53, %10\n"
      "s_add_u32 s53, s53, 33554432\n"
      "buffer_load_dwordx4 %0, s[44:47], s53 offen nt lds\n"
      "buffer_load_dwordx4 %0, s[44:47], s53 offen offset:1024 nt lds\n"
      "v_lshlrev_b32_e32 v0, 16, v112\n"
      "v_lshlrev_b32_e32 v1, 16, v113\n"
      "v_lshlrev_b32_e32 v2, 16, v114\n"
      "v_lshlrev_b32_e32 v3, 16, v115\n"
      "v_and_b32_e32 v112, s57, v112\n"
      "v_and_b32_e32 v113, s57, v113\n"
      "v_and_b32_e32 v114, s57, v114\n"
      "v_and_b32_e32 v115, s57, v115\n"
      "v_mul_f32_e32 v0, s56, v0\n"
      "v_mul_f32_e32 v1, s56, v1\n"
      "v_mul_f32_e32 v2, s56, v2\n"
      "v_mul_f32_e32 v3, s56, v3\n"
      "v_mul_f32_e32 v112, s56, v112\n"
      "v_mul_f32_e32 v113, s56, v113\n"
      "v_mul_f32_e32 v114, s56, v114\n"
      "v_mul_f32_e32 v115, s56, v115\n"
      "v_cvt_pk_bf16_f32 v112, v0, v112\n"
      "v_cvt_pk_bf16_f32 v113, v1, v113\n"
      "v_cvt_pk_bf16_f32 v114, v2, v114\n"
      "v_cvt_pk_bf16_f32 v115, v3, v115\n"
      "HP27_%=:\n"
      "s_memrealtime s[60:61]\n"
      "s_waitcnt lgkmcnt(0)\n"
      "s_sub_u32 s61, s60, s58\n"
      "s_cmp_lt_u32 s61, 56\n"
      "s_cbranch_scc1 HP27_%=\n"
      "s_sub_u32 s61, s60, s62\n"
      "s_cmp_lt_u32 s61, 2241\n"
      "s_cbranch_scc1 HP27_%=\n"
      "s_waitcnt vmcnt(10) lgkmcnt(0)\n"
      "s_barrier\n"
      "s_memrealtime s[58:59]\n"
      "ds_write_b128 %1, v[112:115] offset:45056\n"
      "ds_read_b128 v[136:139], %2 offset:24576\n"
      "s_add_u32 m0, %8, 32768\n"
      "s_add_u32 s52, %14, 114688\n"
      "s_and_b32 s52, s52, 0x1ffff\n"
      "s_add_u32 s52, s52, %10\n"
      "s_add_u32 s52, s52, 33554432\n"
      "buffer_load_dwordx4 %0, s[40:43], s52 offen nt lds\n"
      "buffer_load_dwordx4 %0, s[40:43], s52 offen offset:1024 nt lds\n"
      "s_add_u32 m0, %8, 40960\n"
      "s_add_u32 s53, %14, 106496\n"
      "s_and_b32 s53, s53, 0x1ffff\n"
      "s_add_u32 s53, s53, %10\n"
      "s_add_u32 s53, s53, 33554432\n"
      "buffer_load_dwordx4 %0, s[44:47], s53 offen nt lds\n"
      "buffer_load_dwordx4 %0, s[44:47], s53 offen offset:1024 nt lds\n"
      "HP28_%=:\n"
      "s_memrealtime s[60:61]\n"
      "s_waitcnt lgkmcnt(0)\n"
      "s_sub_u32 s61, s60, s58\n"
      "s_cmp_lt_u32 s61, 56\n"
      "s_cbranch_scc1 HP28_%=\n"
      "s_sub_u32 s61, s60, s62\n"
      "s_cmp_lt_u32 s61, 2312\n"
      "s_cbranch_scc1 HP28_%=\n"
      "s_waitcnt vmcnt(9) lgkmcnt(0)\n"
      "s_barrier\n"
      "s_memrealtime s[58:59]\n"
      "ds_read_b128 v[140:143], %2 offset:28672\n"
      "s_add_u32 m0, %8, 49152\n"
      "s_add_u32 s52, %14, 122880\n"
      "s_and_b32 s52, s52, 0x1ffff\n"
      "s_add_u32 s52, s52, %10\n"
      "s_add_u32 s52, s52, 33554432\n"
      "buffer_load_dwordx4 %0, s[40:43], s52 offen nt lds\n"
      "buffer_load_dwordx4 %0, s[40:43], s52 offen offset:1024 nt lds\n"
      "s_add_u32 s54, %9, 37756928\n"
      "buffer_load_dwordx4 v[144:147], %0, s[36:39], s54 offen nt\n"
      "s_add_u32 m0, %8, 57344\n"
      "s_add_u32 s53, %14, 114688\n"
      "s_and_b32 s53, s53, 0x1ffff\n"
      "s_add_u32 s53, s53, %10\n"
      "s_add_u32 s53, s53, 33554432\n"
      "buffer_load_dwordx4 %0, s[44:47], s53 offen nt lds\n"
      "buffer_load_dwordx4 %0, s[44:47], s53 offen offset:1024 nt lds\n"
      "v_lshlrev_b32_e32 v0, 16, v124\n"
      "v_lshlrev_b32_e32 v1, 16, v125\n"
      "v_lshlrev_b32_e32 v2, 16, v126\n"
      "v_lshlrev_b32_e32 v3, 16, v127\n"
      "v_and_b32_e32 v124, s57, v124\n"
      "v_and_b32_e32 v125, s57, v125\n"
      "v_and_b32_e32 v126, s57, v126\n"
      "v_and_b32_e32 v127, s57, v127\n"
      "v_mul_f32_e32 v0, s56, v0\n"
      "v_mul_f32_e32 v1, s56, v1\n"
      "v_mul_f32_e32 v2, s56, v2\n"
      "v_mul_f32_e32 v3, s56, v3\n"
      "v_mul_f32_e32 v124, s56, v124\n"
      "v_mul_f32_e32 v125, s56, v125\n"
      "v_mul_f32_e32 v126, s56, v126\n"
      "v_mul_f32_e32 v127, s56, v127\n"
      "v_cvt_pk_bf16_f32 v124, v0, v124\n"
      "v_cvt_pk_bf16_f32 v125, v1, v125\n"
      "v_cvt_pk_bf16_f32 v126, v2, v126\n"
      "v_cvt_pk_bf16_f32 v127, v3, v127\n"
      "HP29_%=:\n"
      "s_memrealtime s[60:61]\n"
      "s_waitcnt lgkmcnt(0)\n"
      "s_sub_u32 s61, s60, s58\n"
      "s_cmp_lt_u32 s61, 56\n"
      "s_cbranch_scc1 HP29_%=\n"
      "s_sub_u32 s61, s60, s62\n"
      "s_cmp_lt_u32 s61, 2383\n"
      "s_cbranch_scc1 HP29_%=\n"
      "s_waitcnt vmcnt(9) lgkmcnt(0)\n"
      "s_barrier\n"
      "s_memrealtime s[58:59]\n"
      "ds_read_b128 v[148:151], %2 offset:32768\n"
      "s_add_u32 m0, %8, 0\n"
      "s_add_u32 s52, %14, 0\n"
      "s_and_b32 s52, s52, 0x1ffff\n"
      "s_add_u32 s52, s52, %10\n"
      "s_add_u32 s52, s52, 67108864\n"
      "buffer_load_dwordx4 %0, s[40:43], s52 offen nt lds\n"
      "buffer_load_dwordx4 %0, s[40:43], s52 offen offset:1024 nt lds\n"
      "s_add_u32 s54, %9, 37761024\n"
      "buffer_load_dwordx4 v[152:155], %0, s[36:39], s54 offen nt\n"
      "s_add_u32 m0, %8, 8192\n"
      "s_add_u32 s53, %14, 122880\n"
      "s_and_b32 s53, s53, 0x1ffff\n"
      "s_add_u32 s53, s53, %10\n"
      "s_add_u32 s53, s53, 33554432\n"
      "buffer_load_dwordx4 %0, s[44:47], s53 offen nt lds\n"
      "buffer_load_dwordx4 %0, s[44:47], s53 offen offset:1024 nt lds\n"
      "v_lshlrev_b32_e32 v0, 16, v132\n"
      "v_lshlrev_b32_e32 v1, 16, v133\n"
      "v_lshlrev_b32_e32 v2, 16, v134\n"
      "v_lshlrev_b32_e32 v3, 16, v135\n"
      "v_and_b32_e32 v132, s57, v132\n"
      "v_and_b32_e32 v133, s57, v133\n"
      "v_and_b32_e32 v134, s57, v134\n"
      "v_and_b32_e32 v135, s57, v135\n"
      "v_mul_f32_e32 v0, s56, v0\n"
      "v_mul_f32_e32 v1, s56, v1\n"
      "v_mul_f32_e32 v2, s56, v2\n"
      "v_mul_f32_e32 v3, s56, v3\n"
      "v_mul_f32_e32 v132, s56, v132\n"
      "v_mul_f32_e32 v133, s56, v133\n"
      "v_mul_f32_e32 v134, s56, v134\n"
      "v_mul_f32_e32 v135, s56, v135\n"
      "v_cvt_pk_bf16_f32 v132, v0, v132\n"
      "v_cvt_pk_bf16_f32 v133, v1, v133\n"
      "v_cvt_pk_bf16_f32 v134, v2, v134\n"
      "v_cvt_pk_bf16_f32 v135, v3, v135\n"
      "HP30_%=:\n"
      "s_memrealtime s[60:61]\n"
      "s_waitcnt lgkmcnt(0)\n"
      "s_sub_u32 s61, s60, s58\n"
      "s_cmp_lt_u32 s61, 56\n"
      "s_cbranch_scc1 HP30_%=\n"
      "s_sub_u32 s61, s60, s62\n"
      "s_cmp_lt_u32 s61, 2454\n"
      "s_cbranch_scc1 HP30_%=\n"
      "s_waitcnt vmcnt(10) lgkmcnt(0)\n"
      "s_barrier\n"
      "s_memrealtime s[58:59]\n"
      "ds_read_b128 v[156:159], %2 offset:36864\n"
      "s_add_u32 m0, %8, 16384\n"
      "s_add_u32 s52, %14, 8192\n"
      "s_and_b32 s52, s52, 0x1ffff\n"
      "s_add_u32 s52, s52, %10\n"
      "s_add_u32 s52, s52, 67108864\n"
      "buffer_load_dwordx4 %0, s[40:43], s52 offen nt lds\n"
      "buffer_load_dwordx4 %0, s[40:43], s52 offen offset:1024 nt lds\n"
      "s_add_u32 m0, %8, 24576\n"
      "s_add_u32 s53, %14, 0\n"
      "s_and_b32 s53, s53, 0x1ffff\n"
      "s_add_u32 s53, s53, %10\n"
      "s_add_u32 s53, s53, 67108864\n"
      "buffer_load_dwordx4 %0, s[44:47], s53 offen nt lds\n"
      "buffer_load_dwordx4 %0, s[44:47], s53 offen offset:1024 nt lds\n"
      "HP31_%=:\n"
      "s_memrealtime s[60:61]\n"
      "s_waitcnt lgkmcnt(0)\n"
      "s_sub_u32 s61, s60, s58\n"
      "s_cmp_lt_u32 s61, 56\n"
      "s_cbranch_scc1 HP31_%=\n"
      "s_sub_u32 s61, s60, s62\n"
      "s_cmp_lt_u32 s61, 2525\n"
      "s_cbranch_scc1 HP31_%=\n"
      "s_waitcnt vmcnt(9) lgkmcnt(0)\n"
      "s_barrier\n"
      "s_memrealtime s[58:59]\n"
      "ds_read_b128 v[160:163], %2 offset:40960\n"
      "s_add_u32 m0, %8, 32768\n"
      "s_add_u32 s52, %14, 16384\n"
      "s_and_b32 s52, s52, 0x1ffff\n"
      "s_add_u32 s52, s52, %10\n"
      "s_add_u32 s52, s52, 67108864\n"
      "buffer_load_dwordx4 %0, s[40:43], s52 offen nt lds\n"
      "buffer_load_dwordx4 %0, s[40:43], s52 offen offset:1024 nt lds\n"
      "s_add_u32 s54, %9, 37765120\n"
      "buffer_load_dwordx4 v[164:167], %0, s[36:39], s54 offen nt\n"
      "s_add_u32 m0, %8, 40960\n"
      "s_add_u32 s53, %14, 8192\n"
      "s_and_b32 s53, s53, 0x1ffff\n"
      "s_add_u32 s53, s53, %10\n"
      "s_add_u32 s53, s53, 67108864\n"
      "buffer_load_dwordx4 %0, s[44:47], s53 offen nt lds\n"
      "buffer_load_dwordx4 %0, s[44:47], s53 offen offset:1024 nt lds\n"
      "v_lshlrev_b32_e32 v0, 16, v144\n"
      "v_lshlrev_b32_e32 v1, 16, v145\n"
      "v_lshlrev_b32_e32 v2, 16, v146\n"
      "v_lshlrev_b32_e32 v3, 16, v147\n"
      "v_and_b32_e32 v144, s57, v144\n"
      "v_and_b32_e32 v145, s57, v145\n"
      "v_and_b32_e32 v146, s57, v146\n"
      "v_and_b32_e32 v147, s57, v147\n"
      "v_mul_f32_e32 v0, s56, v0\n"
      "v_mul_f32_e32 v1, s56, v1\n"
      "v_mul_f32_e32 v2, s56, v2\n"
      "v_mul_f32_e32 v3, s56, v3\n"
      "v_mul_f32_e32 v144, s56, v144\n"
      "v_mul_f32_e32 v145, s56, v145\n"
      "v_mul_f32_e32 v146, s56, v146\n"
      "v_mul_f32_e32 v147, s56, v147\n"
      "v_cvt_pk_bf16_f32 v144, v0, v144\n"
      "v_cvt_pk_bf16_f32 v145, v1, v145\n"
      "v_cvt_pk_bf16_f32 v146, v2, v146\n"
      "v_cvt_pk_bf16_f32 v147, v3, v147\n"
      "HP32_%=:\n"
      "s_memrealtime s[60:61]\n"
      "s_waitcnt lgkmcnt(0)\n"
      "s_sub_u32 s61, s60, s58\n"
      "s_cmp_lt_u32 s61, 56\n"
      "s_cbranch_scc1 HP32_%=\n"
      "s_sub_u32 s61, s60, s62\n"
      "s_cmp_lt_u32 s61, 2596\n"
      "s_cbranch_scc1 HP32_%=\n"
      "s_waitcnt vmcnt(9) lgkmcnt(0)\n"
      "s_barrier\n"
      "s_memrealtime s[58:59]\n"
      "ds_write_b128 %1, v[124:127] offset:0\n"
      "ds_read_b128 v[168:171], %2 offset:45056\n"
      "s_add_u32 m0, %8, 49152\n"
      "s_add_u32 s52, %14, 24576\n"
      "s_and_b32 s52, s52, 0x1ffff\n"
      "s_add_u32 s52, s52, %10\n"
      "s_add_u32 s52, s52, 67108864\n"
      "buffer_load_dwordx4 %0, s[40:43], s52 offen nt lds\n"
      "buffer_load_dwordx4 %0, s[40:43], s52 offen offset:1024 nt lds\n"
      "s_add_u32 m0, %8, 57344\n"
      "s_add_u32 s53, %14, 16384\n"
      "s_and_b32 s53, s53, 0x1ffff\n"
      "s_add_u32 s53, s53, %10\n"
      "s_add_u32 s53, s53, 67108864\n"
      "buffer_load_dwordx4 %0, s[44:47], s53 offen nt lds\n"
      "buffer_load_dwordx4 %0, s[44:47], s53 offen offset:1024 nt lds\n"
      "v_lshlrev_b32_e32 v0, 16, v152\n"
      "v_lshlrev_b32_e32 v1, 16, v153\n"
      "v_lshlrev_b32_e32 v2, 16, v154\n"
      "v_lshlrev_b32_e32 v3, 16, v155\n"
      "v_and_b32_e32 v152, s57, v152\n"
      "v_and_b32_e32 v153, s57, v153\n"
      "v_and_b32_e32 v154, s57, v154\n"
      "v_and_b32_e32 v155, s57, v155\n"
      "v_mul_f32_e32 v0, s56, v0\n"
      "v_mul_f32_e32 v1, s56, v1\n"
      "v_mul_f32_e32 v2, s56, v2\n"
      "v_mul_f32_e32 v3, s56, v3\n"
      "v_mul_f32_e32 v152, s56, v152\n"
      "v_mul_f32_e32 v153, s56, v153\n"
      "v_mul_f32_e32 v154, s56, v154\n"
      "v_mul_f32_e32 v155, s56, v155\n"
      "v_cvt_pk_bf16_f32 v152, v0, v152\n"
      "v_cvt_pk_bf16_f32 v153, v1, v153\n"
      "v_cvt_pk_bf16_f32 v154, v2, v154\n"
      "v_cvt_pk_bf16_f32 v155, v3, v155\n"
      "HP33_%=:\n"
      "s_memrealtime s[60:61]\n"
      "s_waitcnt lgkmcnt(0)\n"
      "s_sub_u32 s61, s60, s58\n"
      "s_cmp_lt_u32 s61, 56\n"
      "s_cbranch_scc1 HP33_%=\n"
      "s_sub_u32 s61, s60, s62\n"
      "s_cmp_lt_u32 s61, 2667\n"
      "s_cbranch_scc1 HP33_%=\n"
      "s_waitcnt vmcnt(9) lgkmcnt(0)\n"
      "s_barrier\n"
      "s_memrealtime s[58:59]\n"
      "ds_write_b128 %1, v[132:135] offset:4096\n"
      "s_add_u32 m0, %8, 0\n"
      "s_add_u32 s52, %14, 32768\n"
      "s_and_b32 s52, s52, 0x1ffff\n"
      "s_add_u32 s52, s52, %10\n"
      "s_add_u32 s52, s52, 67108864\n"
      "buffer_load_dwordx4 %0, s[40:43], s52 offen nt lds\n"
      "buffer_load_dwordx4 %0, s[40:43], s52 offen offset:1024 nt lds\n"
      "s_add_u32 s54, %9, 37769216\n"
      "buffer_load_dwordx4 v[172:175], %0, s[36:39], s54 offen nt\n"
      "s_add_u32 s54, %9, 37773312\n"
      "buffer_load_dwordx4 v[176:179], %0, s[36:39], s54 offen nt\n"
      "s_add_u32 s54, %9, 37777408\n"
      "buffer_load_dwordx4 v[180:183], %0, s[36:39], s54 offen nt\n"
      "s_add_u32 m0, %8, 8192\n"
      "s_add_u32 s53, %14, 24576\n"
      "s_and_b32 s53, s53, 0x1ffff\n"
      "s_add_u32 s53, s53, %10\n"
      "s_add_u32 s53, s53, 67108864\n"
      "buffer_load_dwordx4 %0, s[44:47], s53 offen nt lds\n"
      "buffer_load_dwordx4 %0, s[44:47], s53 offen offset:1024 nt lds\n"
      "HP34_%=:\n"
      "s_memrealtime s[60:61]\n"
      "s_waitcnt lgkmcnt(0)\n"
      "s_sub_u32 s61, s60, s58\n"
      "s_cmp_lt_u32 s61, 56\n"
      "s_cbranch_scc1 HP34_%=\n"
      "s_sub_u32 s61, s60, s62\n"
      "s_cmp_lt_u32 s61, 2773\n"
      "s_cbranch_scc1 HP34_%=\n"
      "s_waitcnt vmcnt(11) lgkmcnt(0)\n"
      "s_barrier\n"
      "s_memrealtime s[58:59]\n"
      "ds_write_b128 %1, v[144:147] offset:8192\n"
      "s_add_u32 m0, %8, 16384\n"
      "s_add_u32 s52, %14, 40960\n"
      "s_and_b32 s52, s52, 0x1ffff\n"
      "s_add_u32 s52, s52, %10\n"
      "s_add_u32 s52, s52, 67108864\n"
      "buffer_load_dwordx4 %0, s[40:43], s52 offen nt lds\n"
      "buffer_load_dwordx4 %0, s[40:43], s52 offen offset:1024 nt lds\n"
      "s_add_u32 s54, %9, 37781504\n"
      "buffer_load_dwordx4 v[184:187], %0, s[36:39], s54 offen nt\n"
      "s_add_u32 m0, %8, 24576\n"
      "s_add_u32 s53, %14, 32768\n"
      "s_and_b32 s53, s53, 0x1ffff\n"
      "s_add_u32 s53, s53, %10\n"
      "s_add_u32 s53, s53, 67108864\n"
      "buffer_load_dwordx4 %0, s[44:47], s53 offen nt lds\n"
      "buffer_load_dwordx4 %0, s[44:47], s53 offen offset:1024 nt lds\n"
      "v_lshlrev_b32_e32 v0, 16, v164\n"
      "v_lshlrev_b32_e32 v1, 16, v165\n"
      "v_lshlrev_b32_e32 v2, 16, v166\n"
      "v_lshlrev_b32_e32 v3, 16, v167\n"
      "v_and_b32_e32 v164, s57, v164\n"
      "v_and_b32_e32 v165, s57, v165\n"
      "v_and_b32_e32 v166, s57, v166\n"
      "v_and_b32_e32 v167, s57, v167\n"
      "v_mul_f32_e32 v0, s56, v0\n"
      "v_mul_f32_e32 v1, s56, v1\n"
      "v_mul_f32_e32 v2, s56, v2\n"
      "v_mul_f32_e32 v3, s56, v3\n"
      "v_mul_f32_e32 v164, s56, v164\n"
      "v_mul_f32_e32 v165, s56, v165\n"
      "v_mul_f32_e32 v166, s56, v166\n"
      "v_mul_f32_e32 v167, s56, v167\n"
      "v_cvt_pk_bf16_f32 v164, v0, v164\n"
      "v_cvt_pk_bf16_f32 v165, v1, v165\n"
      "v_cvt_pk_bf16_f32 v166, v2, v166\n"
      "v_cvt_pk_bf16_f32 v167, v3, v167\n"
      "HP35_%=:\n"
      "s_memrealtime s[60:61]\n"
      "s_waitcnt lgkmcnt(0)\n"
      "s_sub_u32 s61, s60, s58\n"
      "s_cmp_lt_u32 s61, 56\n"
      "s_cbranch_scc1 HP35_%=\n"
      "s_sub_u32 s61, s60, s62\n"
      "s_cmp_lt_u32 s61, 2844\n"
      "s_cbranch_scc1 HP35_%=\n"
      "s_waitcnt vmcnt(12) lgkmcnt(0)\n"
      "s_barrier\n"
      "s_memrealtime s[58:59]\n"
      "ds_write_b128 %1, v[152:155] offset:12288\n"
      "s_add_u32 m0, %8, 32768\n"
      "s_add_u32 s52, %14, 49152\n"
      "s_and_b32 s52, s52, 0x1ffff\n"
      "s_add_u32 s52, s52, %10\n"
      "s_add_u32 s52, s52, 67108864\n"
      "buffer_load_dwordx4 %0, s[40:43], s52 offen nt lds\n"
      "buffer_load_dwordx4 %0, s[40:43], s52 offen offset:1024 nt lds\n"
      "s_add_u32 s54, %9, 37785600\n"
      "buffer_load_dwordx4 v[188:191], %0, s[36:39], s54 offen nt\n"
      "s_add_u32 m0, %8, 40960\n"
      "s_add_u32 s53, %14, 40960\n"
      "s_and_b32 s53, s53, 0x1ffff\n"
      "s_add_u32 s53, s53, %10\n"
      "s_add_u32 s53, s53, 67108864\n"
      "buffer_load_dwordx4 %0, s[44:47], s53 offen nt lds\n"
      "buffer_load_dwordx4 %0, s[44:47], s53 offen offset:1024 nt lds\n"
      "HP36_%=:\n"
      "s_memrealtime s[60:61]\n"
      "s_waitcnt lgkmcnt(0)\n"
      "s_sub_u32 s61, s60, s58\n"
      "s_cmp_lt_u32 s61, 56\n"
      "s_cbranch_scc1 HP36_%=\n"
      "s_sub_u32 s61, s60, s62\n"
      "s_cmp_lt_u32 s61, 2915\n"
      "s_cbranch_scc1 HP36_%=\n"
      "s_waitcnt vmcnt(10) lgkmcnt(0)\n"
      "s_barrier\n"
      "s_memrealtime s[58:59]\n"
      "ds_write_b128 %1, v[164:167] offset:16384\n"
      "s_add_u32 m0, %8, 49152\n"
      "s_add_u32 s52, %14, 57344\n"
      "s_and_b32 s52, s52, 0x1ffff\n"
      "s_add_u32 s52, s52, %10\n"
      "s_add_u32 s52, s52, 67108864\n"
      "buffer_load_dwordx4 %0, s[40:43], s52 offen nt lds\n"
      "buffer_load_dwordx4 %0, s[40:43], s52 offen offset:1024 nt lds\n"
      "s_add_u32 m0, %8, 57344\n"
      "s_add_u32 s53, %14, 49152\n"
      "s_and_b32 s53, s53, 0x1ffff\n"
      "s_add_u32 s53, s53, %10\n"
      "s_add_u32 s53, s53, 67108864\n"
      "buffer_load_dwordx4 %0, s[44:47], s53 offen nt lds\n"
      "buffer_load_dwordx4 %0, s[44:47], s53 offen offset:1024 nt lds\n"
      "v_lshlrev_b32_e32 v0, 16, v172\n"
      "v_lshlrev_b32_e32 v1, 16, v173\n"
      "v_lshlrev_b32_e32 v2, 16, v174\n"
      "v_lshlrev_b32_e32 v3, 16, v175\n"
      "v_and_b32_e32 v172, s57, v172\n"
      "v_and_b32_e32 v173, s57, v173\n"
      "v_and_b32_e32 v174, s57, v174\n"
      "v_and_b32_e32 v175, s57, v175\n"
      "v_mul_f32_e32 v0, s56, v0\n"
      "v_mul_f32_e32 v1, s56, v1\n"
      "v_mul_f32_e32 v2, s56, v2\n"
      "v_mul_f32_e32 v3, s56, v3\n"
      "v_mul_f32_e32 v172, s56, v172\n"
      "v_mul_f32_e32 v173, s56, v173\n"
      "v_mul_f32_e32 v174, s56, v174\n"
      "v_mul_f32_e32 v175, s56, v175\n"
      "v_cvt_pk_bf16_f32 v172, v0, v172\n"
      "v_cvt_pk_bf16_f32 v173, v1, v173\n"
      "v_cvt_pk_bf16_f32 v174, v2, v174\n"
      "v_cvt_pk_bf16_f32 v175, v3, v175\n"
      "HP37_%=:\n"
      "s_memrealtime s[60:61]\n"
      "s_waitcnt lgkmcnt(0)\n"
      "s_sub_u32 s61, s60, s58\n"
      "s_cmp_lt_u32 s61, 56\n"
      "s_cbranch_scc1 HP37_%=\n"
      "s_sub_u32 s61, s60, s62\n"
      "s_cmp_lt_u32 s61, 2986\n"
      "s_cbranch_scc1 HP37_%=\n"
      "s_waitcnt vmcnt(9) lgkmcnt(0)\n"
      "s_barrier\n"
      "s_memrealtime s[58:59]\n"
      "ds_write_b128 %1, v[172:175] offset:20480\n"
      "ds_read_b128 v[192:195], %2 offset:0\n"
      "s_add_u32 m0, %8, 0\n"
      "s_add_u32 s52, %14, 65536\n"
      "s_and_b32 s52, s52, 0x1ffff\n"
      "s_add_u32 s52, s52, %10\n"
      "s_add_u32 s52, s52, 67108864\n"
      "buffer_load_dwordx4 %0, s[40:43], s52 offen nt lds\n"
      "buffer_load_dwordx4 %0, s[40:43], s52 offen offset:1024 nt lds\n"
      "s_add_u32 s54, %9, 37789696\n"
      "buffer_load_dwordx4 v[196:199], %0, s[36:39], s54 offen nt\n"
      "s_add_u32 m0, %8, 8192\n"
      "s_add_u32 s53, %14, 57344\n"
      "s_and_b32 s53, s53, 0x1ffff\n"
      "s_add_u32 s53, s53, %10\n"
      "s_add_u32 s53, s53, 67108864\n"
      "buffer_load_dwordx4 %0, s[44:47], s53 offen nt lds\n"
      "buffer_load_dwordx4 %0, s[44:47], s53 offen offset:1024 nt lds\n"
      "v_lshlrev_b32_e32 v0, 16, v176\n"
      "v_lshlrev_b32_e32 v1, 16, v177\n"
      "v_lshlrev_b32_e32 v2, 16, v178\n"
      "v_lshlrev_b32_e32 v3, 16, v179\n"
      "v_and_b32_e32 v176, s57, v176\n"
      "v_and_b32_e32 v177, s57, v177\n"
      "v_and_b32_e32 v178, s57, v178\n"
      "v_and_b32_e32 v179, s57, v179\n"
      "v_mul_f32_e32 v0, s56, v0\n"
      "v_mul_f32_e32 v1, s56, v1\n"
      "v_mul_f32_e32 v2, s56, v2\n"
      "v_mul_f32_e32 v3, s56, v3\n"
      "v_mul_f32_e32 v176, s56, v176\n"
      "v_mul_f32_e32 v177, s56, v177\n"
      "v_mul_f32_e32 v178, s56, v178\n"
      "v_mul_f32_e32 v179, s56, v179\n"
      "v_cvt_pk_bf16_f32 v176, v0, v176\n"
      "v_cvt_pk_bf16_f32 v177, v1, v177\n"
      "v_cvt_pk_bf16_f32 v178, v2, v178\n"
      "v_cvt_pk_bf16_f32 v179, v3, v179\n"
      "HP38_%=:\n"
      "s_memrealtime s[60:61]\n"
      "s_waitcnt lgkmcnt(0)\n"
      "s_sub_u32 s61, s60, s58\n"
      "s_cmp_lt_u32 s61, 56\n"
      "s_cbranch_scc1 HP38_%=\n"
      "s_sub_u32 s61, s60, s62\n"
      "s_cmp_lt_u32 s61, 3057\n"
      "s_cbranch_scc1 HP38_%=\n"
      "s_waitcnt vmcnt(9) lgkmcnt(0)\n"
      "s_barrier\n"
      "s_memrealtime s[58:59]\n"
      "ds_write_b128 %1, v[176:179] offset:24576\n"
      "ds_read_b128 v[200:203], %2 offset:4096\n"
      "s_add_u32 m0, %8, 16384\n"
      "s_add_u32 s52, %14, 73728\n"
      "s_and_b32 s52, s52, 0x1ffff\n"
      "s_add_u32 s52, s52, %10\n"
      "s_add_u32 s52, s52, 67108864\n"
      "buffer_load_dwordx4 %0, s[40:43], s52 offen nt lds\n"
      "buffer_load_dwordx4 %0, s[40:43], s52 offen offset:1024 nt lds\n"
      "s_add_u32 m0, %8, 24576\n"
      "s_add_u32 s53, %14, 65536\n"
      "s_and_b32 s53, s53, 0x1ffff\n"
      "s_add_u32 s53, s53, %10\n"
      "s_add_u32 s53, s53, 67108864\n"
      "buffer_load_dwordx4 %0, s[44:47], s53 offen nt lds\n"
      "buffer_load_dwordx4 %0, s[44:47], s53 offen offset:1024 nt lds\n"
      "v_lshlrev_b32_e32 v0, 16, v180\n"
      "v_lshlrev_b32_e32 v1, 16, v181\n"
      "v_lshlrev_b32_e32 v2, 16, v182\n"
      "v_lshlrev_b32_e32 v3, 16, v183\n"
      "v_and_b32_e32 v180, s57, v180\n"
      "v_and_b32_e32 v181, s57, v181\n"
      "v_and_b32_e32 v182, s57, v182\n"
      "v_and_b32_e32 v183, s57, v183\n"
      "v_mul_f32_e32 v0, s56, v0\n"
      "v_mul_f32_e32 v1, s56, v1\n"
      "v_mul_f32_e32 v2, s56, v2\n"
      "v_mul_f32_e32 v3, s56, v3\n"
      "v_mul_f32_e32 v180, s56, v180\n"
      "v_mul_f32_e32 v181, s56, v181\n"
      "v_mul_f32_e32 v182, s56, v182\n"
      "v_mul_f32_e32 v183, s56, v183\n"
      "v_cvt_pk_bf16_f32 v180, v0, v180\n"
      "v_cvt_pk_bf16_f32 v181, v1, v181\n"
      "v_cvt_pk_bf16_f32 v182, v2, v182\n"
      "v_cvt_pk_bf16_f32 v183, v3, v183\n"
      "HP39_%=:\n"
      "s_memrealtime s[60:61]\n"
      "s_waitcnt lgkmcnt(0)\n"
      "s_sub_u32 s61, s60, s58\n"
      "s_cmp_lt_u32 s61, 56\n"
      "s_cbranch_scc1 HP39_%=\n"
      "s_sub_u32 s61, s60, s62\n"
      "s_cmp_lt_u32 s61, 3128\n"
      "s_cbranch_scc1 HP39_%=\n"
      "s_waitcnt vmcnt(9) lgkmcnt(0)\n"
      "s_barrier\n"
      "s_memrealtime s[58:59]\n"
      "ds_write_b128 %1, v[180:183] offset:28672\n"
      "ds_read_b128 v[204:207], %2 offset:8192\n"
      "s_add_u32 m0, %8, 32768\n"
      "s_add_u32 s52, %14, 81920\n"
      "s_and_b32 s52, s52, 0x1ffff\n"
      "s_add_u32 s52, s52, %10\n"
      "s_add_u32 s52, s52, 67108864\n"
      "buffer_load_dwordx4 %0, s[40:43], s52 offen nt lds\n"
      "buffer_load_dwordx4 %0, s[40:43], s52 offen offset:1024 nt lds\n"
      "s_add_u32 s54, %9, 37793792\n"
      "buffer_load_dwordx4 v[208:211], %0, s[36:39], s54 offen nt\n"
      "s_add_u32 m0, %8, 40960\n"
      "s_add_u32 s53, %14, 73728\n"
      "s_and_b32 s53, s53, 0x1ffff\n"
      "s_add_u32 s53, s53, %10\n"
      "s_add_u32 s53, s53, 67108864\n"
      "buffer_load_dwordx4 %0, s[44:47], s53 offen nt lds\n"
      "buffer_load_dwordx4 %0, s[44:47], s53 offen offset:1024 nt lds\n"
      "v_lshlrev_b32_e32 v0, 16, v184\n"
      "v_lshlrev_b32_e32 v1, 16, v185\n"
      "v_lshlrev_b32_e32 v2, 16, v186\n"
      "v_lshlrev_b32_e32 v3, 16, v187\n"
      "v_and_b32_e32 v184, s57, v184\n"
      "v_and_b32_e32 v185, s57, v185\n"
      "v_and_b32_e32 v186, s57, v186\n"
      "v_and_b32_e32 v187, s57, v187\n"
      "v_mul_f32_e32 v0, s56, v0\n"
      "v_mul_f32_e32 v1, s56, v1\n"
      "v_mul_f32_e32 v2, s56, v2\n"
      "v_mul_f32_e32 v3, s56, v3\n"
      "v_mul_f32_e32 v184, s56, v184\n"
      "v_mul_f32_e32 v185, s56, v185\n"
      "v_mul_f32_e32 v186, s56, v186\n"
      "v_mul_f32_e32 v187, s56, v187\n"
      "v_cvt_pk_bf16_f32 v184, v0, v184\n"
      "v_cvt_pk_bf16_f32 v185, v1, v185\n"
      "v_cvt_pk_bf16_f32 v186, v2, v186\n"
      "v_cvt_pk_bf16_f32 v187, v3, v187\n"
      "HP40_%=:\n"
      "s_memrealtime s[60:61]\n"
      "s_waitcnt lgkmcnt(0)\n"
      "s_sub_u32 s61, s60, s58\n"
      "s_cmp_lt_u32 s61, 56\n"
      "s_cbranch_scc1 HP40_%=\n"
      "s_sub_u32 s61, s60, s62\n"
      "s_cmp_lt_u32 s61, 3199\n"
      "s_cbranch_scc1 HP40_%=\n"
      "s_waitcnt vmcnt(9) lgkmcnt(0)\n"
      "s_barrier\n"
      "s_memrealtime s[58:59]\n"
      "ds_write_b128 %1, v[184:187] offset:32768\n"
      "ds_read_b128 v[212:215], %2 offset:12288\n"
      "s_add_u32 m0, %8, 49152\n"
      "s_add_u32 s52, %14, 90112\n"
      "s_and_b32 s52, s52, 0x1ffff\n"
      "s_add_u32 s52, s52, %10\n"
      "s_add_u32 s52, s52, 67108864\n"
      "buffer_load_dwordx4 %0, s[40:43], s52 offen nt lds\n"
      "buffer_load_dwordx4 %0, s[40:43], s52 offen offset:1024 nt lds\n"
      "s_add_u32 m0, %8, 57344\n"
      "s_add_u32 s53, %14, 81920\n"
      "s_and_b32 s53, s53, 0x1ffff\n"
      "s_add_u32 s53, s53, %10\n"
      "s_add_u32 s53, s53, 67108864\n"
      "buffer_load_dwordx4 %0, s[44:47], s53 offen nt lds\n"
      "buffer_load_dwordx4 %0, s[44:47], s53 offen offset:1024 nt lds\n"
      "s_waitcnt lgkmcnt(0)\n"
      "s_add_u32 s55, %11, 0\n"
      "buffer_store_dwordx4 v[96:99], v4, s[48:51], s55 offen sc1\n"
      "v_lshlrev_b32_e32 v0, 16, v188\n"
      "v_lshlrev_b32_e32 v1, 16, v189\n"
      "v_lshlrev_b32_e32 v2, 16, v190\n"
      "v_lshlrev_b32_e32 v3, 16, v191\n"
      "v_and_b32_e32 v188, s57, v188\n"
      "v_and_b32_e32 v189, s57, v189\n"
      "v_and_b32_e32 v190, s57, v190\n"
      "v_and_b32_e32 v191, s57, v191\n"
      "v_mul_f32_e32 v0, s56, v0\n"
      "v_mul_f32_e32 v1, s56, v1\n"
      "v_mul_f32_e32 v2, s56, v2\n"
      "v_mul_f32_e32 v3, s56, v3\n"
      "v_mul_f32_e32 v188, s56, v188\n"
      "v_mul_f32_e32 v189, s56, v189\n"
      "v_mul_f32_e32 v190, s56, v190\n"
      "v_mul_f32_e32 v191, s56, v191\n"
      "v_cvt_pk_bf16_f32 v188, v0, v188\n"
      "v_cvt_pk_bf16_f32 v189, v1, v189\n"
      "v_cvt_pk_bf16_f32 v190, v2, v190\n"
      "v_cvt_pk_bf16_f32 v191, v3, v191\n"
      "HP41_%=:\n"
      "s_memrealtime s[60:61]\n"
      "s_waitcnt lgkmcnt(0)\n"
      "s_sub_u32 s61, s60, s58\n"
      "s_cmp_lt_u32 s61, 56\n"
      "s_cbranch_scc1 HP41_%=\n"
      "s_sub_u32 s61, s60, s62\n"
      "s_cmp_lt_u32 s61, 3272\n"
      "s_cbranch_scc1 HP41_%=\n"
      "s_waitcnt vmcnt(10) lgkmcnt(0)\n"
      "s_barrier\n"
      "s_memrealtime s[58:59]\n"
      "ds_write_b128 %1, v[188:191] offset:36864\n"
      "ds_read_b128 v[216:219], %2 offset:16384\n"
      "s_add_u32 m0, %8, 0\n"
      "s_add_u32 s52, %14, 98304\n"
      "s_and_b32 s52, s52, 0x1ffff\n"
      "s_add_u32 s52, s52, %10\n"
      "s_add_u32 s52, s52, 67108864\n"
      "buffer_load_dwordx4 %0, s[40:43], s52 offen nt lds\n"
      "buffer_load_dwordx4 %0, s[40:43], s52 offen offset:1024 nt lds\n"
      "s_add_u32 m0, %8, 8192\n"
      "s_add_u32 s53, %14, 90112\n"
      "s_and_b32 s53, s53, 0x1ffff\n"
      "s_add_u32 s53, s53, %10\n"
      "s_add_u32 s53, s53, 67108864\n"
      "buffer_load_dwordx4 %0, s[44:47], s53 offen nt lds\n"
      "buffer_load_dwordx4 %0, s[44:47], s53 offen offset:1024 nt lds\n"
      "v_lshlrev_b32_e32 v0, 16, v196\n"
      "v_lshlrev_b32_e32 v1, 16, v197\n"
      "v_lshlrev_b32_e32 v2, 16, v198\n"
      "v_lshlrev_b32_e32 v3, 16, v199\n"
      "v_and_b32_e32 v196, s57, v196\n"
      "v_and_b32_e32 v197, s57, v197\n"
      "v_and_b32_e32 v198, s57, v198\n"
      "v_and_b32_e32 v199, s57, v199\n"
      "v_mul_f32_e32 v0, s56, v0\n"
      "v_mul_f32_e32 v1, s56, v1\n"
      "v_mul_f32_e32 v2, s56, v2\n"
      "v_mul_f32_e32 v3, s56, v3\n"
      "v_mul_f32_e32 v196, s56, v196\n"
      "v_mul_f32_e32 v197, s56, v197\n"
      "v_mul_f32_e32 v198, s56, v198\n"
      "v_mul_f32_e32 v199, s56, v199\n"
      "v_cvt_pk_bf16_f32 v196, v0, v196\n"
      "v_cvt_pk_bf16_f32 v197, v1, v197\n"
      "v_cvt_pk_bf16_f32 v198, v2, v198\n"
      "v_cvt_pk_bf16_f32 v199, v3, v199\n"
      "HP42_%=:\n"
      "s_memrealtime s[60:61]\n"
      "s_waitcnt lgkmcnt(0)\n"
      "s_sub_u32 s61, s60, s58\n"
      "s_cmp_lt_u32 s61, 56\n"
      "s_cbranch_scc1 HP42_%=\n"
      "s_sub_u32 s61, s60, s62\n"
      "s_cmp_lt_u32 s61, 3345\n"
      "s_cbranch_scc1 HP42_%=\n"
      "s_waitcnt vmcnt(9) lgkmcnt(0)\n"
      "s_barrier\n"
      "s_memrealtime s[58:59]\n"
      "ds_write_b128 %1, v[196:199] offset:40960\n"
      "ds_read_b128 v[220:223], %2 offset:20480\n"
      "s_add_u32 m0, %8, 16384\n"
      "s_add_u32 s52, %14, 106496\n"
      "s_and_b32 s52, s52, 0x1ffff\n"
      "s_add_u32 s52, s52, %10\n"
      "s_add_u32 s52, s52, 67108864\n"
      "buffer_load_dwordx4 %0, s[40:43], s52 offen nt lds\n"
      "buffer_load_dwordx4 %0, s[40:43], s52 offen offset:1024 nt lds\n"
      "s_add_u32 m0, %8, 24576\n"
      "s_add_u32 s53, %14, 98304\n"
      "s_and_b32 s53, s53, 0x1ffff\n"
      "s_add_u32 s53, s53, %10\n"
      "s_add_u32 s53, s53, 67108864\n"
      "buffer_load_dwordx4 %0, s[44:47], s53 offen nt lds\n"
      "buffer_load_dwordx4 %0, s[44:47], s53 offen offset:1024 nt lds\n"
      "s_waitcnt lgkmcnt(0)\n"
      "s_add_u32 s55, %11, 4096\n"
      "buffer_store_dwordx4 v[104:107], v4, s[48:51], s55 offen sc1\n"
      "v_lshlrev_b32_e32 v0, 16, v208\n"
      "v_lshlrev_b32_e32 v1, 16, v209\n"
      "v_lshlrev_b32_e32 v2, 16, v210\n"
      "v_lshlrev_b32_e32 v3, 16, v211\n"
      "v_and_b32_e32 v208, s57, v208\n"
      "v_and_b32_e32 v209, s57, v209\n"
      "v_and_b32_e32 v210, s57, v210\n"
      "v_and_b32_e32 v211, s57, v211\n"
      "v_mul_f32_e32 v0, s56, v0\n"
      "v_mul_f32_e32 v1, s56, v1\n"
      "v_mul_f32_e32 v2, s56, v2\n"
      "v_mul_f32_e32 v3, s56, v3\n"
      "v_mul_f32_e32 v208, s56, v208\n"
      "v_mul_f32_e32 v209, s56, v209\n"
      "v_mul_f32_e32 v210, s56, v210\n"
      "v_mul_f32_e32 v211, s56, v211\n"
      "v_cvt_pk_bf16_f32 v208, v0, v208\n"
      "v_cvt_pk_bf16_f32 v209, v1, v209\n"
      "v_cvt_pk_bf16_f32 v210, v2, v210\n"
      "v_cvt_pk_bf16_f32 v211, v3, v211\n"
      "HP43_%=:\n"
      "s_memrealtime s[60:61]\n"
      "s_waitcnt lgkmcnt(0)\n"
      "s_sub_u32 s61, s60, s58\n"
      "s_cmp_lt_u32 s61, 56\n"
      "s_cbranch_scc1 HP43_%=\n"
      "s_sub_u32 s61, s60, s62\n"
      "s_cmp_lt_u32 s61, 3418\n"
      "s_cbranch_scc1 HP43_%=\n"
      "s_waitcnt vmcnt(10) lgkmcnt(0)\n"
      "s_barrier\n"
      "s_memrealtime s[58:59]\n"
      "ds_write_b128 %1, v[208:211] offset:45056\n"
      "ds_read_b128 v[224:227], %2 offset:24576\n"
      "s_add_u32 m0, %8, 32768\n"
      "s_add_u32 s52, %14, 114688\n"
      "s_and_b32 s52, s52, 0x1ffff\n"
      "s_add_u32 s52, s52, %10\n"
      "s_add_u32 s52, s52, 67108864\n"
      "buffer_load_dwordx4 %0, s[40:43], s52 offen nt lds\n"
      "buffer_load_dwordx4 %0, s[40:43], s52 offen offset:1024 nt lds\n"
      "s_add_u32 m0, %8, 40960\n"
      "s_add_u32 s53, %14, 106496\n"
      "s_and_b32 s53, s53, 0x1ffff\n"
      "s_add_u32 s53, s53, %10\n"
      "s_add_u32 s53, s53, 67108864\n"
      "buffer_load_dwordx4 %0, s[44:47], s53 offen nt lds\n"
      "buffer_load_dwordx4 %0, s[44:47], s53 offen offset:1024 nt lds\n"
      "s_waitcnt lgkmcnt(0)\n"
      "s_add_u32 s55, %11, 8192\n"
      "buffer_store_dwordx4 v[108:111], v4, s[48:51], s55 offen sc1\n"
      "HP44_%=:\n"
      "s_memrealtime s[60:61]\n"
      "s_waitcnt lgkmcnt(0)\n"
      "s_sub_u32 s61, s60, s58\n"
      "s_cmp_lt_u32 s61, 56\n"
      "s_cbranch_scc1 HP44_%=\n"
      "s_sub_u32 s61, s60, s62\n"
      "s_cmp_lt_u32 s61, 3491\n"
      "s_cbranch_scc1 HP44_%=\n"
      "s_waitcnt vmcnt(10) lgkmcnt(0)\n"
      "s_barrier\n"
      "s_memrealtime s[58:59]\n"
      "ds_read_b128 v[228:231], %2 offset:28672\n"
      "s_add_u32 m0, %8, 49152\n"
      "s_add_u32 s52, %14, 122880\n"
      "s_and_b32 s52, s52, 0x1ffff\n"
      "s_add_u32 s52, s52, %10\n"
      "s_add_u32 s52, s52, 67108864\n"
      "buffer_load_dwordx4 %0, s[40:43], s52 offen nt lds\n"
      "buffer_load_dwordx4 %0, s[40:43], s52 offen offset:1024 nt lds\n"
      "s_add_u32 m0, %8, 57344\n"
      "s_add_u32 s53, %14, 114688\n"
      "s_and_b32 s53, s53, 0x1ffff\n"
      "s_add_u32 s53, s53, %10\n"
      "s_add_u32 s53, s53, 67108864\n"
      "buffer_load_dwordx4 %0, s[44:47], s53 offen nt lds\n"
      "buffer_load_dwordx4 %0, s[44:47], s53 offen offset:1024 nt lds\n"
      "HP45_%=:\n"
      "s_memrealtime s[60:61]\n"
      "s_waitcnt lgkmcnt(0)\n"
      "s_sub_u32 s61, s60, s58\n"
      "s_cmp_lt_u32 s61, 56\n"
      "s_cbranch_scc1 HP45_%=\n"
      "s_sub_u32 s61, s60, s62\n"
      "s_cmp_lt_u32 s61, 3564\n"
      "s_cbranch_scc1 HP45_%=\n"
      "s_waitcnt vmcnt(10) lgkmcnt(0)\n"
      "s_barrier\n"
      "s_memrealtime s[58:59]\n"
      "ds_read_b128 v[232:235], %2 offset:32768\n"
      "s_add_u32 m0, %8, 0\n"
      "s_add_u32 s52, %14, 0\n"
      "s_and_b32 s52, s52, 0x1ffff\n"
      "s_add_u32 s52, s52, %10\n"
      "s_add_u32 s52, s52, 100663296\n"
      "buffer_load_dwordx4 %0, s[40:43], s52 offen nt lds\n"
      "buffer_load_dwordx4 %0, s[40:43], s52 offen offset:1024 nt lds\n"
      "s_add_u32 m0, %8, 8192\n"
      "s_add_u32 s53, %14, 122880\n"
      "s_and_b32 s53, s53, 0x1ffff\n"
      "s_add_u32 s53, s53, %10\n"
      "s_add_u32 s53, s53, 67108864\n"
      "buffer_load_dwordx4 %0, s[44:47], s53 offen nt lds\n"
      "buffer_load_dwordx4 %0, s[44:47], s53 offen offset:1024 nt lds\n"
      "s_waitcnt lgkmcnt(0)\n"
      "s_add_u32 s55, %11, 12288\n"
      "buffer_store_dwordx4 v[116:119], v4, s[48:51], s55 offen sc1\n"
      "HP46_%=:\n"
      "s_memrealtime s[60:61]\n"
      "s_waitcnt lgkmcnt(0)\n"
      "s_sub_u32 s61, s60, s58\n"
      "s_cmp_lt_u32 s61, 56\n"
      "s_cbranch_scc1 HP46_%=\n"
      "s_sub_u32 s61, s60, s62\n"
      "s_cmp_lt_u32 s61, 3637\n"
      "s_cbranch_scc1 HP46_%=\n"
      "s_waitcnt vmcnt(10) lgkmcnt(0)\n"
      "s_barrier\n"
      "s_memrealtime s[58:59]\n"
      "ds_read_b128 v[236:239], %2 offset:36864\n"
      "s_add_u32 m0, %8, 16384\n"
      "s_add_u32 s52, %14, 8192\n"
      "s_and_b32 s52, s52, 0x1ffff\n"
      "s_add_u32 s52, s52, %10\n"
      "s_add_u32 s52, s52, 100663296\n"
      "buffer_load_dwordx4 %0, s[40:43], s52 offen nt lds\n"
      "buffer_load_dwordx4 %0, s[40:43], s52 offen offset:1024 nt lds\n"
      "s_add_u32 m0, %8, 24576\n"
      "s_add_u32 s53, %14, 0\n"
      "s_and_b32 s53, s53, 0x1ffff\n"
      "s_add_u32 s53, s53, %10\n"
      "s_add_u32 s53, s53, 100663296\n"
      "buffer_load_dwordx4 %0, s[44:47], s53 offen nt lds\n"
      "buffer_load_dwordx4 %0, s[44:47], s53 offen offset:1024 nt lds\n"
      "HP47_%=:\n"
      "s_memrealtime s[60:61]\n"
      "s_waitcnt lgkmcnt(0)\n"
      "s_sub_u32 s61, s60, s58\n"
      "s_cmp_lt_u32 s61, 56\n"
      "s_cbranch_scc1 HP47_%=\n"
      "s_sub_u32 s61, s60, s62\n"
      "s_cmp_lt_u32 s61, 3710\n"
      "s_cbranch_scc1 HP47_%=\n"
      "s_waitcnt vmcnt(9) lgkmcnt(0)\n"
      "s_barrier\n"
      "s_memrealtime s[58:59]\n"
      "ds_read_b128 v[8:11], %2 offset:40960\n"
      "s_add_u32 m0, %8, 32768\n"
      "s_add_u32 s52, %14, 16384\n"
      "s_and_b32 s52, s52, 0x1ffff\n"
      "s_add_u32 s52, s52, %10\n"
      "s_add_u32 s52, s52, 100663296\n"
      "buffer_load_dwordx4 %0, s[40:43], s52 offen nt lds\n"
      "buffer_load_dwordx4 %0, s[40:43], s52 offen offset:1024 nt lds\n"
      "s_add_u32 m0, %8, 40960\n"
      "s_add_u32 s53, %14, 8192\n"
      "s_and_b32 s53, s53, 0x1ffff\n"
      "s_add_u32 s53, s53, %10\n"
      "s_add_u32 s53, s53, 100663296\n"
      "buffer_load_dwordx4 %0, s[44:47], s53 offen nt lds\n"
      "buffer_load_dwordx4 %0, s[44:47], s53 offen offset:1024 nt lds\n"
      "s_waitcnt lgkmcnt(0)\n"
      "s_add_u32 s55, %11, 16384\n"
      "buffer_store_dwordx4 v[120:123], v4, s[48:51], s55 offen sc1\n"
      "HP48_%=:\n"
      "s_memrealtime s[60:61]\n"
      "s_waitcnt lgkmcnt(0)\n"
      "s_sub_u32 s61, s60, s58\n"
      "s_cmp_lt_u32 s61, 56\n"
      "s_cbranch_scc1 HP48_%=\n"
      "s_sub_u32 s61, s60, s62\n"
      "s_cmp_lt_u32 s61, 3783\n"
      "s_cbranch_scc1 HP48_%=\n"
      "s_waitcnt vmcnt(10) lgkmcnt(0)\n"
      "s_barrier\n"
      "s_memrealtime s[58:59]\n"
      "ds_read_b128 v[12:15], %2 offset:45056\n"
      "s_add_u32 m0, %8, 49152\n"
      "s_add_u32 s52, %14, 24576\n"
      "s_and_b32 s52, s52, 0x1ffff\n"
      "s_add_u32 s52, s52, %10\n"
      "s_add_u32 s52, s52, 100663296\n"
      "buffer_load_dwordx4 %0, s[40:43], s52 offen nt lds\n"
      "buffer_load_dwordx4 %0, s[40:43], s52 offen offset:1024 nt lds\n"
      "s_add_u32 m0, %8, 57344\n"
      "s_add_u32 s53, %14, 16384\n"
      "s_and_b32 s53, s53, 0x1ffff\n"
      "s_add_u32 s53, s53, %10\n"
      "s_add_u32 s53, s53, 100663296\n"
      "buffer_load_dwordx4 %0, s[44:47], s53 offen nt lds\n"
      "buffer_load_dwordx4 %0, s[44:47], s53 offen offset:1024 nt lds\n"
      "s_waitcnt lgkmcnt(0)\n"
      "s_add_u32 s55, %11, 20480\n"
      "buffer_store_dwordx4 v[128:131], v4, s[48:51], s55 offen sc1\n"
      "HP49_%=:\n"
      "s_memrealtime s[60:61]\n"
      "s_waitcnt lgkmcnt(0)\n"
      "s_sub_u32 s61, s60, s58\n"
      "s_cmp_lt_u32 s61, 56\n"
      "s_cbranch_scc1 HP49_%=\n"
      "s_sub_u32 s61, s60, s62\n"
      "s_cmp_lt_u32 s61, 3856\n"
      "s_cbranch_scc1 HP49_%=\n"
      "s_waitcnt vmcnt(10) lgkmcnt(0)\n"
      "s_barrier\n"
      "s_memrealtime s[58:59]\n"
      "s_add_u32 m0, %8, 0\n"
      "s_add_u32 s52, %14, 32768\n"
      "s_and_b32 s52, s52, 0x1ffff\n"
      "s_add_u32 s52, s52, %10\n"
      "s_add_u32 s52, s52, 100663296\n"
      "buffer_load_dwordx4 %0, s[40:43], s52 offen nt lds\n"
      "buffer_load_dwordx4 %0, s[40:43], s52 offen offset:1024 nt lds\n"
      "s_add_u32 m0, %8, 8192\n"
      "s_add_u32 s53, %14, 24576\n"
      "s_and_b32 s53, s53, 0x1ffff\n"
      "s_add_u32 s53, s53, %10\n"
      "s_add_u32 s53, s53, 100663296\n"
      "buffer_load_dwordx4 %0, s[44:47], s53 offen nt lds\n"
      "buffer_load_dwordx4 %0, s[44:47], s53 offen offset:1024 nt lds\n"
      "s_add_u32 s55, %11, 24576\n"
      "buffer_store_dwordx4 v[136:139], v4, s[48:51], s55 offen sc1\n"
      "s_add_u32 s55, %11, 28672\n"
      "buffer_store_dwordx4 v[140:143], v4, s[48:51], s55 offen sc1\n"
      "s_add_u32 s55, %11, 32768\n"
      "buffer_store_dwordx4 v[148:151], v4, s[48:51], s55 offen sc1\n"
      "HP50_%=:\n"
      "s_memrealtime s[60:61]\n"
      "s_waitcnt lgkmcnt(0)\n"
      "s_sub_u32 s61, s60, s58\n"
      "s_cmp_lt_u32 s61, 56\n"
      "s_cbranch_scc1 HP50_%=\n"
      "s_sub_u32 s61, s60, s62\n"
      "s_cmp_lt_u32 s61, 3964\n"
      "s_cbranch_scc1 HP50_%=\n"
      "s_waitcnt vmcnt(13) lgkmcnt(0)\n"
      "s_barrier\n"
      "s_memrealtime s[58:59]\n"
      "s_add_u32 m0, %8, 16384\n"
      "s_add_u32 s52, %14, 40960\n"
      "s_and_b32 s52, s52, 0x1ffff\n"
      "s_add_u32 s52, s52, %10\n"
      "s_add_u32 s52, s52, 100663296\n"
      "buffer_load_dwordx4 %0, s[40:43], s52 offen nt lds\n"
      "buffer_load_dwordx4 %0, s[40:43], s52 offen offset:1024 nt lds\n"
      "s_add_u32 m0, %8, 24576\n"
      "s_add_u32 s53, %14, 32768\n"
      "s_and_b32 s53, s53, 0x1ffff\n"
      "s_add_u32 s53, s53, %10\n"
      "s_add_u32 s53, s53, 100663296\n"
      "buffer_load_dwordx4 %0, s[44:47], s53 offen nt lds\n"
      "buffer_load_dwordx4 %0, s[44:47], s53 offen offset:1024 nt lds\n"
      "HP51_%=:\n"
      "s_memrealtime s[60:61]\n"
      "s_waitcnt lgkmcnt(0)\n"
      "s_sub_u32 s61, s60, s58\n"
      "s_cmp_lt_u32 s61, 56\n"
      "s_cbranch_scc1 HP51_%=\n"
      "s_sub_u32 s61, s60, s62\n"
      "s_cmp_lt_u32 s61, 4037\n"
      "s_cbranch_scc1 HP51_%=\n"
      "s_waitcnt vmcnt(12) lgkmcnt(0)\n"
      "s_barrier\n"
      "s_memrealtime s[58:59]\n"
      "s_add_u32 m0, %8, 32768\n"
      "s_add_u32 s52, %14, 49152\n"
      "s_and_b32 s52, s52, 0x1ffff\n"
      "s_add_u32 s52, s52, %10\n"
      "s_add_u32 s52, s52, 100663296\n"
      "buffer_load_dwordx4 %0, s[40:43], s52 offen nt lds\n"
      "buffer_load_dwordx4 %0, s[40:43], s52 offen offset:1024 nt lds\n"
      "s_add_u32 m0, %8, 40960\n"
      "s_add_u32 s53, %14, 40960\n"
      "s_and_b32 s53, s53, 0x1ffff\n"
      "s_add_u32 s53, s53, %10\n"
      "s_add_u32 s53, s53, 100663296\n"
      "buffer_load_dwordx4 %0, s[44:47], s53 offen nt lds\n"
      "buffer_load_dwordx4 %0, s[44:47], s53 offen offset:1024 nt lds\n"
      "s_add_u32 s55, %11, 36864\n"
      "buffer_store_dwordx4 v[156:159], v4, s[48:51], s55 offen sc1\n"
      "HP52_%=:\n"
      "s_memrealtime s[60:61]\n"
      "s_waitcnt lgkmcnt(0)\n"
      "s_sub_u32 s61, s60, s58\n"
      "s_cmp_lt_u32 s61, 56\n"
      "s_cbranch_scc1 HP52_%=\n"
      "s_sub_u32 s61, s60, s62\n"
      "s_cmp_lt_u32 s61, 4110\n"
      "s_cbranch_scc1 HP52_%=\n"
      "s_waitcnt vmcnt(12) lgkmcnt(0)\n"
      "s_barrier\n"
      "s_memrealtime s[58:59]\n"
      "ds_read_b128 v[16:19], %2 offset:0\n"
      "s_add_u32 m0, %8, 49152\n"
      "s_add_u32 s52, %14, 57344\n"
      "s_and_b32 s52, s52, 0x1ffff\n"
      "s_add_u32 s52, s52, %10\n"
      "s_add_u32 s52, s52, 100663296\n"
      "buffer_load_dwordx4 %0, s[40:43], s52 offen nt lds\n"
      "buffer_load_dwordx4 %0, s[40:43], s52 offen offset:1024 nt lds\n"
      "s_add_u32 m0, %8, 57344\n"
      "s_add_u32 s53, %14, 49152\n"
      "s_and_b32 s53, s53, 0x1ffff\n"
      "s_add_u32 s53, s53, %10\n"
      "s_add_u32 s53, s53, 100663296\n"
      "buffer_load_dwordx4 %0, s[44:47], s53 offen nt lds\n"
      "buffer_load_dwordx4 %0, s[44:47], s53 offen offset:1024 nt lds\n"
      "s_waitcnt lgkmcnt(0)\n"
      "s_add_u32 s55, %11, 40960\n"
      "buffer_store_dwordx4 v[160:163], v4, s[48:51], s55 offen sc1\n"
      "HP53_%=:\n"
      "s_memrealtime s[60:61]\n"
      "s_waitcnt lgkmcnt(0)\n"
      "s_sub_u32 s61, s60, s58\n"
      "s_cmp_lt_u32 s61, 56\n"
      "s_cbranch_scc1 HP53_%=\n"
      "s_sub_u32 s61, s60, s62\n"
      "s_cmp_lt_u32 s61, 4183\n"
      "s_cbranch_scc1 HP53_%=\n"
      "s_waitcnt vmcnt(10) lgkmcnt(0)\n"
      "s_barrier\n"
      "s_memrealtime s[58:59]\n"
      "ds_read_b128 v[20:23], %2 offset:4096\n"
      "s_add_u32 m0, %8, 0\n"
      "s_add_u32 s52, %14, 65536\n"
      "s_and_b32 s52, s52, 0x1ffff\n"
      "s_add_u32 s52, s52, %10\n"
      "s_add_u32 s52, s52, 100663296\n"
      "buffer_load_dwordx4 %0, s[40:43], s52 offen nt lds\n"
      "buffer_load_dwordx4 %0, s[40:43], s52 offen offset:1024 nt lds\n"
      "s_add_u32 m0, %8, 8192\n"
      "s_add_u32 s53, %14, 57344\n"
      "s_and_b32 s53, s53, 0x1ffff\n"
      "s_add_u32 s53, s53, %10\n"
      "s_add_u32 s53, s53, 100663296\n"
      "buffer_load_dwordx4 %0, s[44:47], s53 offen nt lds\n"
      "buffer_load_dwordx4 %0, s[44:47], s53 offen offset:1024 nt lds\n"
      "HP54_%=:\n"
      "s_memrealtime s[60:61]\n"
      "s_waitcnt lgkmcnt(0)\n"
      "s_sub_u32 s61, s60, s58\n"
      "s_cmp_lt_u32 s61, 56\n"
      "s_cbranch_scc1 HP54_%=\n"
      "s_sub_u32 s61, s60, s62\n"
      "s_cmp_lt_u32 s61, 4256\n"
      "s_cbranch_scc1 HP54_%=\n"
      "s_waitcnt vmcnt(10) lgkmcnt(0)\n"
      "s_barrier\n"
      "s_memrealtime s[58:59]\n"
      "ds_read_b128 v[24:27], %2 offset:8192\n"
      "s_add_u32 m0, %8, 16384\n"
      "s_add_u32 s52, %14, 73728\n"
      "s_and_b32 s52, s52, 0x1ffff\n"
      "s_add_u32 s52, s52, %10\n"
      "s_add_u32 s52, s52, 100663296\n"
      "buffer_load_dwordx4 %0, s[40:43], s52 offen nt lds\n"
      "buffer_load_dwordx4 %0, s[40:43], s52 offen offset:1024 nt lds\n"
      "s_add_u32 m0, %8, 24576\n"
      "s_add_u32 s53, %14, 65536\n"
      "s_and_b32 s53, s53, 0x1ffff\n"
      "s_add_u32 s53, s53, %10\n"
      "s_add_u32 s53, s53, 100663296\n"
      "buffer_load_dwordx4 %0, s[44:47], s53 offen nt lds\n"
      "buffer_load_dwordx4 %0, s[44:47], s53 offen offset:1024 nt lds\n"
      "s_waitcnt lgkmcnt(0)\n"
      "s_add_u32 s55, %11, 45056\n"
      "buffer_store_dwordx4 v[168:171], v4, s[48:51], s55 offen sc1\n"
      "HP55_%=:\n"
      "s_memrealtime s[60:61]\n"
      "s_waitcnt lgkmcnt(0)\n"
      "s_sub_u32 s61, s60, s58\n"
      "s_cmp_lt_u32 s61, 56\n"
      "s_cbranch_scc1 HP55_%=\n"
      "s_sub_u32 s61, s60, s62\n"
      "s_cmp_lt_u32 s61, 4329\n"
      "s_cbranch_scc1 HP55_%=\n"
      "s_waitcnt vmcnt(10) lgkmcnt(0)\n"
      "s_barrier\n"
      "s_memrealtime s[58:59]\n"
      "ds_read_b128 v[28:31], %2 offset:12288\n"
      "s_add_u32 m0, %8, 32768\n"
      "s_add_u32 s52, %14, 81920\n"
      "s_and_b32 s52, s52, 0x1ffff\n"
      "s_add_u32 s52, s52, %10\n"
      "s_add_u32 s52, s52, 100663296\n"
      "buffer_load_dwordx4 %0, s[40:43], s52 offen nt lds\n"
      "buffer_load_dwordx4 %0, s[40:43], s52 offen offset:1024 nt lds\n"
      "s_add_u32 m0, %8, 40960\n"
      "s_add_u32 s53, %14, 73728\n"
      "s_and_b32 s53, s53, 0x1ffff\n"
      "s_add_u32 s53, s53, %10\n"
      "s_add_u32 s53, s53, 100663296\n"
      "buffer_load_dwordx4 %0, s[44:47], s53 offen nt lds\n"
      "buffer_load_dwordx4 %0, s[44:47], s53 offen offset:1024 nt lds\n"
      "HP56_%=:\n"
      "s_memrealtime s[60:61]\n"
      "s_waitcnt lgkmcnt(0)\n"
      "s_sub_u32 s61, s60, s58\n"
      "s_cmp_lt_u32 s61, 56\n"
      "s_cbranch_scc1 HP56_%=\n"
      "s_sub_u32 s61, s60, s62\n"
      "s_cmp_lt_u32 s61, 4402\n"
      "s_cbranch_scc1 HP56_%=\n"
      "s_waitcnt vmcnt(9) lgkmcnt(0)\n"
      "s_barrier\n"
      "s_memrealtime s[58:59]\n"
      "ds_read_b128 v[32:35], %2 offset:16384\n"
      "s_add_u32 m0, %8, 49152\n"
      "s_add_u32 s52, %14, 90112\n"
      "s_and_b32 s52, s52, 0x1ffff\n"
      "s_add_u32 s52, s52, %10\n"
      "s_add_u32 s52, s52, 100663296\n"
      "buffer_load_dwordx4 %0, s[40:43], s52 offen nt lds\n"
      "buffer_load_dwordx4 %0, s[40:43], s52 offen offset:1024 nt lds\n"
      "s_add_u32 m0, %8, 57344\n"
      "s_add_u32 s53, %14, 81920\n"
      "s_and_b32 s53, s53, 0x1ffff\n"
      "s_add_u32 s53, s53, %10\n"
      "s_add_u32 s53, s53, 100663296\n"
      "buffer_load_dwordx4 %0, s[44:47], s53 offen nt lds\n"
      "buffer_load_dwordx4 %0, s[44:47], s53 offen offset:1024 nt lds\n"
      "s_waitcnt lgkmcnt(0)\n"
      "s_add_u32 s55, %11, 12582912\n"
      "buffer_store_dwordx4 v[192:195], v4, s[48:51], s55 offen sc1\n"
      "HP57_%=:\n"
      "s_memrealtime s[60:61]\n"
      "s_waitcnt lgkmcnt(0)\n"
      "s_sub_u32 s61, s60, s58\n"
      "s_cmp_lt_u32 s61, 56\n"
      "s_cbranch_scc1 HP57_%=\n"
      "s_sub_u32 s61, s60, s62\n"
      "s_cmp_lt_u32 s61, 4475\n"
      "s_cbranch_scc1 HP57_%=\n"
      "s_waitcnt vmcnt(10) lgkmcnt(0)\n"
      "s_barrier\n"
      "s_memrealtime s[58:59]\n"
      "ds_read_b128 v[36:39], %2 offset:20480\n"
      "s_add_u32 m0, %8, 0\n"
      "s_add_u32 s52, %14, 98304\n"
      "s_and_b32 s52, s52, 0x1ffff\n"
      "s_add_u32 s52, s52, %10\n"
      "s_add_u32 s52, s52, 100663296\n"
      "buffer_load_dwordx4 %0, s[40:43], s52 offen nt lds\n"
      "buffer_load_dwordx4 %0, s[40:43], s52 offen offset:1024 nt lds\n"
      "s_add_u32 m0, %8, 8192\n"
      "s_add_u32 s53, %14, 90112\n"
      "s_and_b32 s53, s53, 0x1ffff\n"
      "s_add_u32 s53, s53, %10\n"
      "s_add_u32 s53, s53, 100663296\n"
      "buffer_load_dwordx4 %0, s[44:47], s53 offen nt lds\n"
      "buffer_load_dwordx4 %0, s[44:47], s53 offen offset:1024 nt lds\n"
      "s_waitcnt lgkmcnt(0)\n"
      "s_add_u32 s55, %11, 12587008\n"
      "buffer_store_dwordx4 v[200:203], v4, s[48:51], s55 offen sc1\n"
      "HP58_%=:\n"
      "s_memrealtime s[60:61]\n"
      "s_waitcnt lgkmcnt(0)\n"
      "s_sub_u32 s61, s60, s58\n"
      "s_cmp_lt_u32 s61, 56\n"
      "s_cbranch_scc1 HP58_%=\n"
      "s_sub_u32 s61, s60, s62\n"
      "s_cmp_lt_u32 s61, 4548\n"
      "s_cbranch_scc1 HP58_%=\n"
      "s_waitcnt vmcnt(10) lgkmcnt(0)\n"
      "s_barrier\n"
      "s_memrealtime s[58:59]\n"
      "ds_read_b128 v[40:43], %2 offset:24576\n"
      "s_add_u32 m0, %8, 16384\n"
      "s_add_u32 s52, %14, 106496\n"
      "s_and_b32 s52, s52, 0x1ffff\n"
      "s_add_u32 s52, s52, %10\n"
      "s_add_u32 s52, s52, 100663296\n"
      "buffer_load_dwordx4 %0, s[40:43], s52 offen nt lds\n"
      "buffer_load_dwordx4 %0, s[40:43], s52 offen offset:1024 nt lds\n"
      "s_add_u32 m0, %8, 24576\n"
      "s_add_u32 s53, %14, 98304\n"
      "s_and_b32 s53, s53, 0x1ffff\n"
      "s_add_u32 s53, s53, %10\n"
      "s_add_u32 s53, s53, 100663296\n"
      "buffer_load_dwordx4 %0, s[44:47], s53 offen nt lds\n"
      "buffer_load_dwordx4 %0, s[44:47], s53 offen offset:1024 nt lds\n"
      "HP59_%=:\n"
      "s_memrealtime s[60:61]\n"
      "s_waitcnt lgkmcnt(0)\n"
      "s_sub_u32 s61, s60, s58\n"
      "s_cmp_lt_u32 s61, 56\n"
      "s_cbranch_scc1 HP59_%=\n"
      "s_sub_u32 s61, s60, s62\n"
      "s_cmp_lt_u32 s61, 4621\n"
      "s_cbranch_scc1 HP59_%=\n"
      "s_waitcnt vmcnt(10) lgkmcnt(0)\n"
      "s_barrier\n"
      "s_memrealtime s[58:59]\n"
      "ds_read_b128 v[44:47], %2 offset:28672\n"
      "s_add_u32 m0, %8, 32768\n"
      "s_add_u32 s52, %14, 114688\n"
      "s_and_b32 s52, s52, 0x1ffff\n"
      "s_add_u32 s52, s52, %10\n"
      "s_add_u32 s52, s52, 100663296\n"
      "buffer_load_dwordx4 %0, s[40:43], s52 offen nt lds\n"
      "buffer_load_dwordx4 %0, s[40:43], s52 offen offset:1024 nt lds\n"
      "s_add_u32 m0, %8, 40960\n"
      "s_add_u32 s53, %14, 106496\n"
      "s_and_b32 s53, s53, 0x1ffff\n"
      "s_add_u32 s53, s53, %10\n"
      "s_add_u32 s53, s53, 100663296\n"
      "buffer_load_dwordx4 %0, s[44:47], s53 offen nt lds\n"
      "buffer_load_dwordx4 %0, s[44:47], s53 offen offset:1024 nt lds\n"
      "s_waitcnt lgkmcnt(0)\n"
      "s_add_u32 s55, %11, 12591104\n"
      "buffer_store_dwordx4 v[204:207], v4, s[48:51], s55 offen sc1\n"
      "HP60_%=:\n"
      "s_memrealtime s[60:61]\n"
      "s_waitcnt lgkmcnt(0)\n"
      "s_sub_u32 s61, s60, s58\n"
      "s_cmp_lt_u32 s61, 56\n"
      "s_cbranch_scc1 HP60_%=\n"
      "s_sub_u32 s61, s60, s62\n"
      "s_cmp_lt_u32 s61, 4694\n"
      "s_cbranch_scc1 HP60_%=\n"
      "s_waitcnt vmcnt(10) lgkmcnt(0)\n"
      "s_barrier\n"
      "s_memrealtime s[58:59]\n"
      "ds_read_b128 v[48:51], %2 offset:32768\n"
      "s_add_u32 m0, %8, 49152\n"
      "s_add_u32 s52, %14, 122880\n"
      "s_and_b32 s52, s52, 0x1ffff\n"
      "s_add_u32 s52, s52, %10\n"
      "s_add_u32 s52, s52, 100663296\n"
      "buffer_load_dwordx4 %0, s[40:43], s52 offen nt lds\n"
      "buffer_load_dwordx4 %0, s[40:43], s52 offen offset:1024 nt lds\n"
      "s_add_u32 m0, %8, 57344\n"
      "s_add_u32 s53, %14, 114688\n"
      "s_and_b32 s53, s53, 0x1ffff\n"
      "s_add_u32 s53, s53, %10\n"
      "s_add_u32 s53, s53, 100663296\n"
      "buffer_load_dwordx4 %0, s[44:47], s53 offen nt lds\n"
      "buffer_load_dwordx4 %0, s[44:47], s53 offen offset:1024 nt lds\n"
      "s_waitcnt lgkmcnt(0)\n"
      "s_add_u32 s55, %11, 12595200\n"
      "buffer_store_dwordx4 v[212:215], v4, s[48:51], s55 offen sc1\n"
      "HP61_%=:\n"
      "s_memrealtime s[60:61]\n"
      "s_waitcnt lgkmcnt(0)\n"
      "s_sub_u32 s61, s60, s58\n"
      "s_cmp_lt_u32 s61, 56\n"
      "s_cbranch_scc1 HP61_%=\n"
      "s_sub_u32 s61, s60, s62\n"
      "s_cmp_lt_u32 s61, 4767\n"
      "s_cbranch_scc1 HP61_%=\n"
      "s_waitcnt vmcnt(10) lgkmcnt(0)\n"
      "s_barrier\n"
      "s_memrealtime s[58:59]\n"
      "ds_read_b128 v[52:55], %2 offset:36864\n"
      "s_add_u32 m0, %8, 8192\n"
      "s_add_u32 s53, %14, 122880\n"
      "s_and_b32 s53, s53, 0x1ffff\n"
      "s_add_u32 s53, s53, %10\n"
      "s_add_u32 s53, s53, 100663296\n"
      "buffer_load_dwordx4 %0, s[44:47], s53 offen nt lds\n"
      "buffer_load_dwordx4 %0, s[44:47], s53 offen offset:1024 nt lds\n"
      "HP62_%=:\n"
      "s_memrealtime s[60:61]\n"
      "s_waitcnt lgkmcnt(0)\n"
      "s_sub_u32 s61, s60, s58\n"
      "s_cmp_lt_u32 s61, 56\n"
      "s_cbranch_scc1 HP62_%=\n"
      "s_sub_u32 s61, s60, s62\n"
      "s_cmp_lt_u32 s61, 4840\n"
      "s_cbranch_scc1 HP62_%=\n"
      "s_waitcnt vmcnt(8) lgkmcnt(0)\n"
      "s_barrier\n"
      "s_memrealtime s[58:59]\n"
      "ds_read_b128 v[56:59], %2 offset:40960\n"
      "s_waitcnt lgkmcnt(0)\n"
      "s_add_u32 s55, %11, 12599296\n"
      "buffer_store_dwordx4 v[216:219], v4, s[48:51], s55 offen sc1\n"
      "s_add_u32 s55, %11, 12603392\n"
      "buffer_store_dwordx4 v[220:223], v4, s[48:51], s55 offen sc1\n"
      "s_add_u32 s55, %11, 12607488\n"
      "buffer_store_dwordx4 v[224:227], v4, s[48:51], s55 offen sc1\n"
      "s_add_u32 s55, %11, 12611584\n"
      "buffer_store_dwordx4 v[228:231], v4, s[48:51], s55 offen sc1\n"
      "s_add_u32 s55, %11, 12615680\n"
      "buffer_store_dwordx4 v[232:235], v4, s[48:51], s55 offen sc1\n"
      "s_add_u32 s55, %11, 12619776\n"
      "buffer_store_dwordx4 v[236:239], v4, s[48:51], s55 offen sc1\n"
      "s_add_u32 s55, %11, 12623872\n"
      "buffer_store_dwordx4 v[8:11], v4, s[48:51], s55 offen sc1\n"
      "HP63_%=:\n"
      "s_memrealtime s[60:61]\n"
      "s_waitcnt lgkmcnt(0)\n"
      "s_sub_u32 s61, s60, s58\n"
      "s_cmp_lt_u32 s61, 56\n"
      "s_cbranch_scc1 HP63_%=\n"
      "s_sub_u32 s61, s60, s62\n"
      "s_cmp_lt_u32 s61, 4913\n"
      "s_cbranch_scc1 HP63_%=\n"
      "s_waitcnt vmcnt(10) lgkmcnt(0)\n"
      "s_barrier\n"
      "s_memrealtime s[58:59]\n"
      "ds_read_b128 v[60:63], %2 offset:45056\n"
      "s_waitcnt lgkmcnt(0)\n"
      "s_add_u32 s55, %11, 12627968\n"
      "buffer_store_dwordx4 v[12:15], v4, s[48:51], s55 offen sc1\n"
      "s_add_u32 s55, %11, 25165824\n"
      "buffer_store_dwordx4 v[16:19], v4, s[48:51], s55 offen sc1\n"
      "s_add_u32 s55, %11, 25169920\n"
      "buffer_store_dwordx4 v[20:23], v4, s[48:51], s55 offen sc1\n"
      "s_add_u32 s55, %11, 25174016\n"
      "buffer_store_dwordx4 v[24:27], v4, s[48:51], s55 offen sc1\n"
      "s_add_u32 s55, %11, 25178112\n"
      "buffer_store_dwordx4 v[28:31], v4, s[48:51], s55 offen sc1\n"
      "s_add_u32 s55, %11, 25182208\n"
      "buffer_store_dwordx4 v[32:35], v4, s[48:51], s55 offen sc1\n"
      "HP64_%=:\n"
      "s_memrealtime s[60:61]\n"
      "s_waitcnt lgkmcnt(0)\n"
      "s_sub_u32 s61, s60, s58\n"
      "s_cmp_lt_u32 s61, 56\n"
      "s_cbranch_scc1 HP64_%=\n"
      "s_sub_u32 s61, s60, s62\n"
      "s_cmp_lt_u32 s61, 4986\n"
      "s_cbranch_scc1 HP64_%=\n"
      "s_waitcnt vmcnt(13) lgkmcnt(0)\n"
      "s_barrier\n"
      "s_memrealtime s[58:59]\n"
      "s_add_u32 s55, %11, 25186304\n"
      "buffer_store_dwordx4 v[36:39], v4, s[48:51], s55 offen sc1\n"
      "s_add_u32 s55, %11, 25190400\n"
      "buffer_store_dwordx4 v[40:43], v4, s[48:51], s55 offen sc1\n"
      "s_add_u32 s55, %11, 25194496\n"
      "buffer_store_dwordx4 v[44:47], v4, s[48:51], s55 offen sc1\n"
      "s_add_u32 s55, %11, 25198592\n"
      "buffer_store_dwordx4 v[48:51], v4, s[48:51], s55 offen sc1\n"
      "s_add_u32 s55, %11, 25202688\n"
      "buffer_store_dwordx4 v[52:55], v4, s[48:51], s55 offen sc1\n"
      "s_add_u32 s55, %11, 25206784\n"
      "buffer_store_dwordx4 v[56:59], v4, s[48:51], s55 offen sc1\n"
      "s_add_u32 s55, %11, 25210880\n"
      "buffer_store_dwordx4 v[60:63], v4, s[48:51], s55 offen sc1\n"
      "s_barrier\n"
      "ds_read_b128 v[64:67], %2 offset:0\n"
      "ds_read_b128 v[68:71], %2 offset:12288\n"
      "ds_read_b128 v[72:75], %2 offset:24576\n"
      "ds_read_b128 v[76:79], %2 offset:36864\n"
      "s_waitcnt lgkmcnt(0)\n"
      "s_add_u32 s55, %11, 37748736\n"
      "buffer_store_dwordx4 v[64:67], v4, s[48:51], s55 offen sc1\n"
      "s_add_u32 s55, %11, 37761024\n"
      "buffer_store_dwordx4 v[68:71], v4, s[48:51], s55 offen sc1\n"
      "s_add_u32 s55, %11, 37773312\n"
      "buffer_store_dwordx4 v[72:75], v4, s[48:51], s55 offen sc1\n"
      "s_add_u32 s55, %11, 37785600\n"
      "buffer_store_dwordx4 v[76:79], v4, s[48:51], s55 offen sc1\n"
      "s_barrier\n"
      "ds_read_b128 v[80:83], %2 offset:4096\n"
      "ds_read_b128 v[84:87], %2 offset:16384\n"
      "ds_read_b128 v[88:91], %2 offset:28672\n"
      "ds_read_b128 v[92:95], %2 offset:40960\n"
      "s_waitcnt lgkmcnt(0)\n"
      "s_add_u32 s55, %11, 37752832\n"
      "buffer_store_dwordx4 v[80:83], v4, s[48:51], s55 offen sc1\n"
      "s_add_u32 s55, %11, 37765120\n"
      "buffer_store_dwordx4 v[84:87], v4, s[48:51], s55 offen sc1\n"
      "s_add_u32 s55, %11, 37777408\n"
      "buffer_store_dwordx4 v[88:91], v4, s[48:51], s55 offen sc1\n"
      "s_add_u32 s55, %11, 37789696\n"
      "buffer_store_dwordx4 v[92:95], v4, s[48:51], s55 offen sc1\n"
      "s_barrier\n"
      "ds_read_b128 v[100:103], %2 offset:8192\n"
      "ds_read_b128 v[112:115], %2 offset:20480\n"
      "ds_read_b128 v[124:127], %2 offset:32768\n"
      "ds_read_b128 v[132:135], %2 offset:45056\n"
      "s_waitcnt lgkmcnt(0)\n"
      "s_add_u32 s55, %11, 37756928\n"
      "buffer_store_dwordx4 v[100:103], v4, s[48:51], s55 offen sc1\n"
      "s_add_u32 s55, %11, 37769216\n"
      "buffer_store_dwordx4 v[112:115], v4, s[48:51], s55 offen sc1\n"
      "s_add_u32 s55, %11, 37781504\n"
      "buffer_store_dwordx4 v[124:127], v4, s[48:51], s55 offen sc1\n"
      "s_add_u32 s55, %11, 37793792\n"
      "buffer_store_dwordx4 v[132:135], v4, s[48:51], s55 offen sc1\n"
      "s_waitcnt vmcnt(0) lgkmcnt(0)\n"
      "s_barrier\n"
      "HEND%=:\n"
      :
      : "v"(0 ? lane * 16u : f_voff(lane, hw)), "v"(ldsb + QST + hw * 1024u + lane * 16u), "v"(or0), "v"(or0 ^ 64u),
        "s"((unsigned long long)Q & am), "s"((unsigned long long)K & am), "s"((unsigned long long)V & am), "s"((unsigned long long)O & am),
        "s"(ldsb + hw * 2048u),
        "s"(h0 * QBYTES + hw * 1024u), "s"(h0 * KBYTES + hw * 2048u), "s"(h0 * QBYTES + hw * (1 ? 1024u : 12288u)), "s"(hw * 8192u),
        "s"((((__builtin_amdgcn_workgroup_id_x() >> 3) * 13u + __builtin_amdgcn_workgroup_id_x() * 4u) & 31u) * 70u / 32u),
        "s"(((2 == 2 ? (bid >> 3) : bid + (bid >> 4)) & 15u) * 8192u)
      : "memory", "scc", "vcc", "v0", "v1", "v2", "v3", "v4", "v5", "v6", "v7", "v8", "v9", "v10", "v11", "v12", "v13", "v14", "v15", "v16", "v17", "v18", "v19", "v20", "v21", "v22", "v23", "v24", "v25", "v26", "v27", "v28", "v29", "v30", "v31", "v32", "v33", "v34", "v35", "v36", "v37", "v38", "v39", "v40", "v41", "v42", "v43", "v44", "v45", "v46", "v47", "v48", "v49", "v50", "v51", "v52", "v53", "v54", "v55", "v56", "v57", "v58", "v59", "v60", "v61", "v62", "v63", "v64", "v65", "v66", "v67", "v68", "v69", "v70", "v71", "v72", "v73", "v74", "v75", "v76", "v77", "v78", "v79", "v80", "v81", "v82", "v83", "v84", "v85", "v86", "v87", "v88", "v89", "v90", "v91", "v92", "v93", "v94", "v95", "v96", "v97", "v98", "v99", "v100", "v101", "v102", "v103", "v104", "v105", "v106", "v107", "v108", "v109", "v110", "v111", "v112", "v113", "v114", "v115", "v116", "v117", "v118", "v119", "v120", "v121", "v122", "v123", "v124", "v125", "v126", "v127", "v128", "v129", "v130", "v131", "v132", "v133", "v134", "v135", "v136", "v137", "v138", "v139", "v140", "v141", "v142", "v143", "v144", "v145", "v146", "v147", "v148", "v149", "v150", "v151", "v152", "v153", "v154", "v155", "v156", "v157", "v158", "v159", "v160", "v161", "v162", "v163", "v164", "v165", "v166", "v167", "v168", "v169", "v170", "v171", "v172", "v173", "v174", "v175", "v176", "v177", "v178", "v179", "v180", "v181", "v182", "v183", "v184", "v185", "v186", "v187", "v188", "v189", "v190", "v191", "v192", "v193", "v194", "v195", "v196", "v197", "v198", "v199", "v200", "v201", "v202", "v203", "v204", "v205", "v206", "v207", "v208", "v209", "v210", "v211", "v212", "v213", "v214", "v215", "v216", "v217", "v218", "v219", "v220", "v221", "v222", "v223", "v224", "v225", "v226", "v227", "v228", "v229", "v230", "v231", "v232", "v233", "v234", "v235", "v236", "v237", "v238", "v239", "s36", "s37", "s38", "s39", "s40", "s41", "s42", "s43", "s44", "s45", "s46", "s47", "s48", "s49", "s50", "s51", "s52", "s53", "s54", "s55", "s56", "s57", "s58", "s59", "s60", "s61", "s62", "s63");
    return;
  }
  unsigned flag;
  {
    const unsigned lane = tid & 63u, i16 = lane & 15u, g = lane >> 4;
    const unsigned vb = ldsb + f_vbase(lane);
    asm volatile(
      "s_mov_b64 s[52:53], %7\n"
      "s_mov_b32 s54, 0x3000000\n"
      "s_mov_b32 s55, 0x20000\n"
      "s_movk_i32 s56, 0\n"
      "buffer_load_dwordx4 v[96:99], %6, s[52:55], s56 offen offset:0 nt\n"
      "buffer_load_dwordx4 v[100:103], %6, s[52:55], s56 offen offset:64 nt\n"
      "buffer_load_dwordx4 v[104:107], %6, s[52:55], s56 offen offset:128 nt\n"
      "buffer_load_dwordx4 v[108:111], %6, s[52:55], s56 offen offset:192 nt\n"
      "s_movk_i32 s56, 4096\n"
      "buffer_load_dwordx4 v[112:115], %6, s[52:55], s56 offen offset:0 nt\n"
      "buffer_load_dwordx4 v[116:119], %6, s[52:55], s56 offen offset:64 nt\n"
      "buffer_load_dwordx4 v[120:123], %6, s[52:55], s56 offen offset:128 nt\n"
      "buffer_load_dwordx4 v[124:127], %6, s[52:55], s56 offen offset:192 nt\n"
      "s_movk_i32 s56, 8192\n"
      "buffer_load_dwordx4 v[128:131], %6, s[52:55], s56 offen offset:0 nt\n"
      "buffer_load_dwordx4 v[132:135], %6, s[52:55], s56 offen offset:64 nt\n"
      "buffer_load_dwordx4 v[136:139], %6, s[52:55], s56 offen offset:128 nt\n"
      "buffer_load_dwordx4 v[140:143], %6, s[52:55], s56 offen offset:192 nt\n"
      "s_mov_b32 s56, 0x3e0293ee\n"
      "s_mov_b32 s57, 0xffff0000\n"
      "s_mov_b32 %0, 0\n"
      "s_mov_b32 s43, 1\n"
      "s_mov_b32 s44, 0x5d800000\n"
      "s_mov_b32 s40, 4\n"
      "s_barrier\n"
      "s_getpc_b64 s[52:53]\n"
      "CPC%=:\n"
      "s_and_b32 s53, s53, 0xffff\n"
      "s_mov_b32 s54, CEND%=-CPC%=\n"
      "s_mov_b32 s55, 0x20000\n"
      "v_mbcnt_lo_u32_b32 v0, -1, 0\n"
      "v_mbcnt_hi_u32_b32 v0, -1, v0\n"
      "v_lshlrev_b32_e32 v0, 7, v0\n"
      "buffer_load_dword v0, v0, s[52:55], %8 offen\n"
      "s_waitcnt lgkmcnt(0)\n"
      "s_barrier\n"
      "ds_read_b128 v[216:219], %1 offset:0\n"
      "ds_read_b128 v[220:223], %1 offset:1024\n"
      "ds_read_b128 v[224:227], %1 offset:256\n"
      "ds_read_b128 v[228:231], %1 offset:1280\n"
      "s_waitcnt vmcnt(9)\n"
      "v_lshlrev_b32_e32 v144, 16, v96\n"
      "v_lshlrev_b32_e32 v145, 16, v97\n"
      "v_lshlrev_b32_e32 v146, 16, v98\n"
      "v_lshlrev_b32_e32 v147, 16, v99\n"
      "v_lshlrev_b32_e32 v148, 16, v100\n"
      "v_lshlrev_b32_e32 v149, 16, v101\n"
      "v_lshlrev_b32_e32 v150, 16, v102\n"
      "v_lshlrev_b32_e32 v151, 16, v103\n"
      "v_and_b32_e32 v96, s57, v96\n"
      "v_and_b32_e32 v97, s57, v97\n"
      "v_and_b32_e32 v98, s57, v98\n"
      "v_and_b32_e32 v99, s57, v99\n"
      "v_and_b32_e32 v100, s57, v100\n"
      "v_and_b32_e32 v101, s57, v101\n"
      "v_and_b32_e32 v102, s57, v102\n"
      "v_and_b32_e32 v103, s57, v103\n"
      "v_mul_f32_e32 v144, s56, v144\n"
      "v_mul_f32_e32 v145, s56, v145\n"
      "v_mul_f32_e32 v146, s56, v146\n"
      "v_mul_f32_e32 v147, s56, v147\n"
      "v_mul_f32_e32 v148, s56, v148\n"
      "v_mul_f32_e32 v149, s56, v149\n"
      "v_mul_f32_e32 v150, s56, v150\n"
      "v_mul_f32_e32 v151, s56, v151\n"
      "v_mul_f32_e32 v96, s56, v96\n"
      "v_mul_f32_e32 v97, s56, v97\n"
      "v_mul_f32_e32 v98, s56, v98\n"
      "v_mul_f32_e32 v99, s56, v99\n"
      "v_mul_f32_e32 v100, s56, v100\n"
      "v_mul_f32_e32 v101, s56, v101\n"
      "v_mul_f32_e32 v102, s56, v102\n"
      "v_mul_f32_e32 v103, s56, v103\n"
      "v_cvt_pk_bf16_f32 v96, v144, v96\n"
      "v_cvt_pk_bf16_f32 v97, v145, v97\n"
      "v_cvt_pk_bf16_f32 v98, v146, v98\n"
      "v_cvt_pk_bf16_f32 v99, v147, v99\n"
      "v_cvt_pk_bf16_f32 v100, v148, v100\n"
      "v_cvt_pk_bf16_f32 v101, v149, v101\n"
      "v_cvt_pk_bf16_f32 v102, v150, v102\n"
      "v_cvt_pk_bf16_f32 v103, v151, v103\n"
      "v_lshlrev_b32_e32 v144, 16, v104\n"
      "v_lshlrev_b32_e32 v145, 16, v105\n"
      "v_lshlrev_b32_e32 v146, 16, v106\n"
      "v_lshlrev_b32_e32 v147, 16, v107\n"
      "v_lshlrev_b32_e32 v148, 16, v108\n"
      "v_lshlrev_b32_e32 v149, 16, v109\n"
      "v_lshlrev_b32_e32 v150, 16, v110\n"
      "v_lshlrev_b32_e32 v151, 16, v111\n"
      "v_and_b32_e32 v104, s57, v104\n"
      "v_and_b32_e32 v105, s57, v105\n"
      "v_and_b32_e32 v106, s57, v106\n"
      "v_and_b32_e32 v107, s57, v107\n"
      "v_and_b32_e32 v108, s57, v108\n"
      "v_and_b32_e32 v109, s57, v109\n"
      "v_and_b32_e32 v110, s57, v110\n"
      "v_and_b32_e32 v111, s57, v111\n"
      "v_mul_f32_e32 v144, s56, v144\n"
      "v_mul_f32_e32 v145, s56, v145\n"
      "v_mul_f32_e32 v146, s56, v146\n"
      "v_mul_f32_e32 v147, s56, v147\n"
      "v_mul_f32_e32 v148, s56, v148\n"
      "v_mul_f32_e32 v149, s56, v149\n"
      "v_mul_f32_e32 v150, s56, v150\n"
      "v_mul_f32_e32 v151, s56, v151\n"
      "v_mul_f32_e32 v104, s56, v104\n"
      "v_mul_f32_e32 v105, s56, v105\n"
      "v_mul_f32_e32 v106, s56, v106\n"
      "v_mul_f32_e32 v107, s56, v107\n"
      "v_mul_f32_e32 v108, s56, v108\n"
      "v_mul_f32_e32 v109, s56, v109\n"
      "v_mul_f32_e32 v110, s56, v110\n"
      "v_mul_f32_e32 v111, s56, v111\n"
      "v_cvt_pk_bf16_f32 v104, v144, v104\n"
      "v_cvt_pk_bf16_f32 v105, v145, v105\n"
      "v_cvt_pk_bf16_f32 v106, v146, v106\n"
      "v_cvt_pk_bf16_f32 v107, v147, v107\n"
      "v_cvt_pk_bf16_f32 v108, v148, v108\n"
      "v_cvt_pk_bf16_f32 v109, v149, v109\n"
      "v_cvt_pk_bf16_f32 v110, v150, v110\n"
      "v_cvt_pk_bf16_f32 v111, v151, v111\n"
      "s_waitcnt vmcnt(5)\n"
      "v_lshlrev_b32_e32 v144, 16, v112\n"
      "v_lshlrev_b32_e32 v145, 16, v113\n"
      "v_lshlrev_b32_e32 v146, 16, v114\n"
      "v_lshlrev_b32_e32 v147, 16, v115\n"
      "v_lshlrev_b32_e32 v148, 16, v116\n"
      "v_lshlrev_b32_e32 v149, 16, v117\n"
      "v_lshlrev_b32_e32 v150, 16, v118\n"
      "v_lshlrev_b32_e32 v151, 16, v119\n"
      "v_and_b32_e32 v112, s57, v112\n"
      "v_and_b32_e32 v113, s57, v113\n"
      "v_and_b32_e32 v114, s57, v114\n"
      "v_and_b32_e32 v115, s57, v115\n"
      "v_and_b32_e32 v116, s57, v116\n"
      "v_and_b32_e32 v117, s57, v117\n"
      "v_and_b32_e32 v118, s57, v118\n"
      "v_and_b32_e32 v119, s57, v119\n"
      "v_mul_f32_e32 v144, s56, v144\n"
      "v_mul_f32_e32 v145, s56, v145\n"
      "v_mul_f32_e32 v146, s56, v146\n"
      "v_mul_f32_e32 v147, s56, v147\n"
      "v_mul_f32_e32 v148, s56, v148\n"
      "v_mul_f32_e32 v149, s56, v149\n"
      "v_mul_f32_e32 v150, s56, v150\n"
      "v_mul_f32_e32 v151, s56, v151\n"
      "v_mul_f32_e32 v112, s56, v112\n"
      "v_mul_f32_e32 v113, s56, v113\n"
      "v_mul_f32_e32 v114, s56, v114\n"
      "v_mul_f32_e32 v115, s56, v115\n"
      "v_mul_f32_e32 v116, s56, v116\n"
      "v_mul_f32_e32 v117, s56, v117\n"
      "v_mul_f32_e32 v118, s56, v118\n"
      "v_mul_f32_e32 v119, s56, v119\n"
      "v_cvt_pk_bf16_f32 v112, v144, v112\n"
      "v_cvt_pk_bf16_f32 v113, v145, v113\n"
      "v_cvt_pk_bf16_f32 v114, v146, v114\n"
      "v_cvt_pk_bf16_f32 v115, v147, v115\n"
      "v_cvt_pk_bf16_f32 v116, v148, v116\n"
      "v_cvt_pk_bf16_f32 v117, v149, v117\n"
      "v_cvt_pk_bf16_f32 v118, v150, v118\n"
      "v_cvt_pk_bf16_f32 v119, v151, v119\n"
      "v_lshlrev_b32_e32 v144, 16, v120\n"
      "v_lshlrev_b32_e32 v145, 16, v121\n"
      "v_lshlrev_b32_e32 v146, 16, v122\n"
      "v_lshlrev_b32_e32 v147, 16, v123\n"
      "v_lshlrev_b32_e32 v148, 16, v124\n"
      "v_lshlrev_b32_e32 v149, 16, v125\n"
      "v_lshlrev_b32_e32 v150, 16, v126\n"
      "v_lshlrev_b32_e32 v151, 16, v127\n"
      "v_and_b32_e32 v120, s57, v120\n"
      "v_and_b32_e32 v121, s57, v121\n"
      "v_and_b32_e32 v122, s57, v122\n"
      "v_and_b32_e32 v123, s57, v123\n"
      "v_and_b32_e32 v124, s57, v124\n"
      "v_and_b32_e32 v125, s57, v125\n"
      "v_and_b32_e32 v126, s57, v126\n"
      "v_and_b32_e32 v127, s57, v127\n"
      "v_mul_f32_e32 v144, s56, v144\n"
      "v_mul_f32_e32 v145, s56, v145\n"
      "v_mul_f32_e32 v146, s56, v146\n"
      "v_mul_f32_e32 v147, s56, v147\n"
      "v_mul_f32_e32 v148, s56, v148\n"
      "v_mul_f32_e32 v149, s56, v149\n"
      "v_mul_f32_e32 v150, s56, v150\n"
      "v_mul_f32_e32 v151, s56, v151\n"
      "v_mul_f32_e32 v120, s56, v120\n"
      "v_mul_f32_e32 v121, s56, v121\n"
      "v_mul_f32_e32 v122, s56, v122\n"
      "v_mul_f32_e32 v123, s56, v123\n"
      "v_mul_f32_e32 v124, s56, v124\n"
      "v_mul_f32_e32 v125, s56, v125\n"
      "v_mul_f32_e32 v126, s56, v126\n"
      "v_mul_f32_e32 v127, s56, v127\n"
      "v_cvt_pk_bf16_f32 v120, v144, v120\n"
      "v_cvt_pk_bf16_f32 v121, v145, v121\n"
      "v_cvt_pk_bf16_f32 v122, v146, v122\n"
      "v_cvt_pk_bf16_f32 v123, v147, v123\n"
      "v_cvt_pk_bf16_f32 v124, v148, v124\n"
      "v_cvt_pk_bf16_f32 v125, v149, v125\n"
      "v_cvt_pk_bf16_f32 v126, v150, v126\n"
      "v_cvt_pk_bf16_f32 v127, v151, v127\n"
      "s_waitcnt vmcnt(1)\n"
      "v_lshlrev_b32_e32 v144, 16, v128\n"
      "v_lshlrev_b32_e32 v145, 16, v129\n"
      "v_lshlrev_b32_e32 v146, 16, v130\n"
      "v_lshlrev_b32_e32 v147, 16, v131\n"
      "v_lshlrev_b32_e32 v148, 16, v132\n"
      "v_lshlrev_b32_e32 v149, 16, v133\n"
      "v_lshlrev_b32_e32 v150, 16, v134\n"
      "v_lshlrev_b32_e32 v151, 16, v135\n"
      "v_and_b32_e32 v128, s57, v128\n"
      "v_and_b32_e32 v129, s57, v129\n"
      "v_and_b32_e32 v130, s57, v130\n"
      "v_and_b32_e32 v131, s57, v131\n"
      "v_and_b32_e32 v132, s57, v132\n"
      "v_and_b32_e32 v133, s57, v133\n"
      "v_and_b32_e32 v134, s57, v134\n"
      "v_and_b32_e32 v135, s57, v135\n"
      "v_mul_f32_e32 v144, s56, v144\n"
      "v_mul_f32_e32 v145, s56, v145\n"
      "v_mul_f32_e32 v146, s56, v146\n"
      "v_mul_f32_e32 v147, s56, v147\n"
      "v_mul_f32_e32 v148, s56, v148\n"
      "v_mul_f32_e32 v149, s56, v149\n"
      "v_mul_f32_e32 v150, s56, v150\n"
      "v_mul_f32_e32 v151, s56, v151\n"
      "v_mul_f32_e32 v128, s56, v128\n"
      "v_mul_f32_e32 v129, s56, v129\n"
      "v_mul_f32_e32 v130, s56, v130\n"
      "v_mul_f32_e32 v131, s56, v131\n"
      "v_mul_f32_e32 v132, s56, v132\n"
      "v_mul_f32_e32 v133, s56, v133\n"
      "v_mul_f32_e32 v134, s56, v134\n"
      "v_mul_f32_e32 v135, s56, v135\n"
      "v_cvt_pk_bf16_f32 v128, v144, v128\n"
      "v_cvt_pk_bf16_f32 v129, v145, v129\n"
      "v_cvt_pk_bf16_f32 v130, v146, v130\n"
      "v_cvt_pk_bf16_f32 v131, v147, v131\n"
      "v_cvt_pk_bf16_f32 v132, v148, v132\n"
      "v_cvt_pk_bf16_f32 v133, v149, v133\n"
      "v_cvt_pk_bf16_f32 v134, v150, v134\n"
      "v_cvt_pk_bf16_f32 v135, v151, v135\n"
      "v_lshlrev_b32_e32 v144, 16, v136\n"
      "v_lshlrev_b32_e32 v145, 16, v137\n"
      "v_lshlrev_b32_e32 v146, 16, v138\n"
      "v_lshlrev_b32_e32 v147, 16, v139\n"
      "v_lshlrev_b32_e32 v148, 16, v140\n"
      "v_lshlrev_b32_e32 v149, 16, v141\n"
      "v_lshlrev_b32_e32 v150, 16, v142\n"
      "v_lshlrev_b32_e32 v151, 16, v143\n"
      "v_and_b32_e32 v136, s57, v136\n"
      "v_and_b32_e32 v137, s57, v137\n"
      "v_and_b32_e32 v138, s57, v138\n"
      "v_and_b32_e32 v139, s57, v139\n"
      "v_and_b32_e32 v140, s57, v140\n"
      "v_and_b32_e32 v141, s57, v141\n"
      "v_and_b32_e32 v142, s57, v142\n"
      "v_and_b32_e32 v143, s57, v143\n"
      "v_mul_f32_e32 v144, s56, v144\n"
      "v_mul_f32_e32 v145, s56, v145\n"
      "v_mul_f32_e32 v146, s56, v146\n"
      "v_mul_f32_e32 v147, s56, v147\n"
      "v_mul_f32_e32 v148, s56, v148\n"
      "v_mul_f32_e32 v149, s56, v149\n"
      "v_mul_f32_e32 v150, s56, v150\n"
      "v_mul_f32_e32 v151, s56, v151\n"
      "v_mul_f32_e32 v136, s56, v136\n"
      "v_mul_f32_e32 v137, s56, v137\n"
      "v_mul_f32_e32 v138, s56, v138\n"
      "v_mul_f32_e32 v139, s56, v139\n"
      "v_mul_f32_e32 v140, s56, v140\n"
      "v_mul_f32_e32 v141, s56, v141\n"
      "v_mul_f32_e32 v142, s56, v142\n"
      "v_mul_f32_e32 v143, s56, v143\n"
      "v_cvt_pk_bf16_f32 v136, v144, v136\n"
      "v_cvt_pk_bf16_f32 v137, v145, v137\n"
      "v_cvt_pk_bf16_f32 v138, v146, v138\n"
      "v_cvt_pk_bf16_f32 v139, v147, v139\n"
      "v_cvt_pk_bf16_f32 v140, v148, v140\n"
      "v_cvt_pk_bf16_f32 v141, v149, v141\n"
      "v_cvt_pk_bf16_f32 v142, v150, v142\n"
      "v_cvt_pk_bf16_f32 v143, v151, v143\n"
      "s_waitcnt lgkmcnt(2)\n"
      "v_mfma_f32_16x16x32_bf16 v[144:147], v[216:219], v[96:99], 0\n"
      "v_mfma_f32_16x16x32_bf16 v[148:151], v[216:219], v[112:115], 0\n"
      "v_mfma_f32_16x16x32_bf16 v[152:155], v[216:219], v[128:131], 0\n"
      "ds_read_b128 v[216:219], %1 offset:512\n"
      "v_mfma_f32_16x16x32_bf16 v[164:167], v[220:223], v[128:131], 0\n"
      "v_mfma_f32_16x16x32_bf16 v[160:163], v[220:223], v[112:115], 0\n"
      "v_mfma_f32_16x16x32_bf16 v[156:159], v[220:223], v[96:99], 0\n"
      "ds_read_b128 v[220:223], %1 offset:1536\n"
      "s_waitcnt lgkmcnt(2)\n"
      "v_mfma_f32_16x16x32_bf16 v[144:147], v[224:227], v[100:103], v[144:147]\n"
      "v_mfma_f32_16x16x32_bf16 v[148:151], v[224:227], v[116:119], v[148:151]\n"
      "v_mfma_f32_16x16x32_bf16 v[152:155], v[224:227], v[132:135], v[152:155]\n"
      "ds_read_b128 v[224:227], %1 offset:768\n"
      "v_mfma_f32_16x16x32_bf16 v[164:167], v[228:231], v[132:135], v[164:167]\n"
      "v_mfma_f32_16x16x32_bf16 v[160:163], v[228:231], v[116:119], v[160:163]\n"
      "v_mfma_f32_16x16x32_bf16 v[156:159], v[228:231], v[100:103], v[156:159]\n"
      "ds_read_b128 v[228:231], %1 offset:1792\n"
      "s_waitcnt lgkmcnt(2)\n"
      "v_mfma_f32_16x16x32_bf16 v[144:147], v[216:219], v[104:107], v[144:147]\n"
      "v_mfma_f32_16x16x32_bf16 v[148:151], v[216:219], v[120:123], v[148:151]\n"
      "v_mfma_f32_16x16x32_bf16 v[152:155], v[216:219], v[136:139], v[152:155]\n"
      "v_mfma_f32_16x16x32_bf16 v[164:167], v[220:223], v[136:139], v[164:167]\n"
      "v_mfma_f32_16x16x32_bf16 v[160:163], v[220:223], v[120:123], v[160:163]\n"
      "v_mfma_f32_16x16x32_bf16 v[156:159], v[220:223], v[104:107], v[156:159]\n"
      "s_waitcnt lgkmcnt(0)\n"
      "v_mfma_f32_16x16x32_bf16 v[144:147], v[224:227], v[108:111], v[144:147]\n"
      "v_mfma_f32_16x16x32_bf16 v[148:151], v[224:227], v[124:127], v[148:151]\n"
      "v_mfma_f32_16x16x32_bf16 v[152:155], v[224:227], v[140:143], v[152:155]\n"
      "v_mfma_f32_16x16x32_bf16 v[164:167], v[228:231], v[140:143], v[164:167]\n"
      "v_mfma_f32_16x16x32_bf16 v[160:163], v[228:231], v[124:127], v[160:163]\n"
      "v_mfma_f32_16x16x32_bf16 v[156:159], v[228:231], v[108:111], v[156:159]\n"
      "s_waitcnt lgkmcnt(0)\n"
      "s_barrier\n"
      "ds_read_b128 v[216:219], %1 offset:16384\n"
      "ds_read_b128 v[220:223], %1 offset:17408\n"
      "ds_read_b128 v[224:227], %1 offset:16640\n"
      "ds_read_b128 v[228:231], %1 offset:17664\n"
      "v_max3_f32 v204, v144, v145, v146\n"
      "v_max3_f32 v208, v148, v149, v150\n"
      "v_max3_f32 v212, v152, v153, v154\n"
      "v_max3_f32 v205, v147, v156, v157\n"
      "v_max3_f32 v204, v204, v158, v159\n"
      "v_max3_f32 v209, v151, v160, v161\n"
      "v_max3_f32 v208, v208, v162, v163\n"
      "v_max3_f32 v213, v155, v164, v165\n"
      "v_max3_f32 v212, v212, v166, v167\n"
      "v_max_f32_e32 v204, v204, v205\n"
      "v_max_f32_e32 v208, v208, v209\n"
      "v_max_f32_e32 v212, v212, v213\n"
      "v_mov_b32_e32 v205, v204\n"
      "v_mov_b32_e32 v209, v208\n"
      "v_mov_b32_e32 v213, v212\n"
      "v_permlane16_swap_b32_e32 v204, v205\n"
      "v_permlane16_swap_b32_e32 v208, v209\n"
      "v_permlane16_swap_b32_e32 v212, v213\n"
      "v_max_f32_e32 v204, v204, v205\n"
      "v_max_f32_e32 v208, v208, v209\n"
      "v_max_f32_e32 v212, v212, v213\n"
      "v_mov_b32_e32 v205, v204\n"
      "v_mov_b32_e32 v209, v208\n"
      "v_mov_b32_e32 v213, v212\n"
      "v_permlane32_swap_b32_e32 v204, v205\n"
      "v_permlane32_swap_b32_e32 v208, v209\n"
      "v_permlane32_swap_b32_e32 v212, v213\n"
      "v_max_f32_e32 v204, v204, v205\n"
      "v_max_f32_e32 v208, v208, v209\n"
      "v_max_f32_e32 v212, v212, v213\n"
      "v_sub_f32_e32 v144, v144, v204\n"
      "v_sub_f32_e32 v145, v145, v204\n"
      "v_sub_f32_e32 v146, v146, v204\n"
      "v_sub_f32_e32 v147, v147, v204\n"
      "v_sub_f32_e32 v148, v148, v208\n"
      "v_sub_f32_e32 v149, v149, v208\n"
      "v_sub_f32_e32 v150, v150, v208\n"
      "v_sub_f32_e32 v151, v151, v208\n"
      "v_sub_f32_e32 v152, v152, v212\n"
      "v_sub_f32_e32 v153, v153, v212\n"
      "v_sub_f32_e32 v154, v154, v212\n"
      "v_sub_f32_e32 v155, v155, v212\n"
      "v_sub_f32_e32 v156, v156, v204\n"
      "v_sub_f32_e32 v157, v157, v204\n"
      "v_sub_f32_e32 v158, v158, v204\n"
      "v_sub_f32_e32 v159, v159, v204\n"
      "v_sub_f32_e32 v160, v160, v208\n"
      "v_sub_f32_e32 v161, v161, v208\n"
      "v_sub_f32_e32 v162, v162, v208\n"
      "v_sub_f32_e32 v163, v163, v208\n"
      "v_sub_f32_e32 v164, v164, v212\n"
      "v_sub_f32_e32 v165, v165, v212\n"
      "v_sub_f32_e32 v166, v166, v212\n"
      "v_sub_f32_e32 v167, v167, v212\n"
      "v_sub_f32_e32 v207, 0, v204\n"
      "v_sub_f32_e32 v206, 0, v204\n"
      "v_sub_f32_e32 v205, 0, v204\n"
      "v_sub_f32_e32 v204, 0, v204\n"
      "v_sub_f32_e32 v211, 0, v208\n"
      "v_sub_f32_e32 v210, 0, v208\n"
      "v_sub_f32_e32 v209, 0, v208\n"
      "v_sub_f32_e32 v208, 0, v208\n"
      "v_sub_f32_e32 v215, 0, v212\n"
      "v_sub_f32_e32 v214, 0, v212\n"
      "v_sub_f32_e32 v213, 0, v212\n"
      "v_sub_f32_e32 v212, 0, v212\n"
      "v_exp_f32_e32 v144, v144\n"
      "v_exp_f32_e32 v145, v145\n"
      "s_nop 0\n"
      "v_add_f32_e32 v232, v144, v145\n"
      "v_exp_f32_e32 v146, v146\n"
      "v_exp_f32_e32 v147, v147\n"
      "v_add_f32_e32 v232, v232, v146\n"
      "v_add_f32_e32 v232, v232, v147\n"
      "v_exp_f32_e32 v148, v148\n"
      "v_exp_f32_e32 v149, v149\n"
      "s_nop 0\n"
      "v_add_f32_e32 v233, v148, v149\n"
      "v_exp_f32_e32 v150, v150\n"
      "v_exp_f32_e32 v151, v151\n"
      "v_add_f32_e32 v233, v233, v150\n"
      "v_add_f32_e32 v233, v233, v151\n"
      "v_exp_f32_e32 v152, v152\n"
      "v_exp_f32_e32 v153, v153\n"
      "s_nop 0\n"
      "v_add_f32_e32 v234, v152, v153\n"
      "v_exp_f32_e32 v154, v154\n"
      "v_exp_f32_e32 v155, v155\n"
      "v_add_f32_e32 v234, v234, v154\n"
      "v_add_f32_e32 v234, v234, v155\n"
      "v_exp_f32_e32 v156, v156\n"
      "v_exp_f32_e32 v157, v157\n"
      "v_add_f32_e32 v232, v232, v156\n"
      "v_add_f32_e32 v232, v232, v157\n"
      "s_waitcnt vmcnt(0)\n"
      "s_waitcnt lgkmcnt(2)\n"
      "v_mfma_f32_16x16x32_bf16 v[168:171], v[216:219], v[96:99], v[204:207]\n"
      "v_exp_f32_e32 v158, v158\n"
      "v_mfma_f32_16x16x32_bf16 v[172:175], v[216:219], v[112:115], v[208:211]\n"
      "v_cvt_pk_bf16_f32 v192, v144, v145\n"
      "v_cvt_pk_bf16_f32 v193, v146, v147\n"
      "v_mfma_f32_16x16x32_bf16 v[176:179], v[216:219], v[128:131], v[212:215]\n"
      "ds_read_b128 v[216:219], %1 offset:16896\n"
      "v_exp_f32_e32 v159, v159\n"
      "v_mfma_f32_16x16x32_bf16 v[188:191], v[220:223], v[128:131], v[212:215]\n"
      "v_cvt_pk_bf16_f32 v196, v148, v149\n"
      "v_cvt_pk_bf16_f32 v197, v150, v151\n"
      "v_mfma_f32_16x16x32_bf16 v[184:187], v[220:223], v[112:115], v[208:211]\n"
      "v_exp_f32_e32 v160, v160\n"
      "v_cvt_pk_bf16_f32 v200, v152, v153\n"
      "v_mfma_f32_16x16x32_bf16 v[180:183], v[220:223], v[96:99], v[204:207]\n"
      "ds_read_b128 v[220:223], %1 offset:17920\n"
      "s_waitcnt lgkmcnt(2)\n"
      "v_mfma_f32_16x16x32_bf16 v[168:171], v[224:227], v[100:103], v[168:171]\n"
      "v_cvt_pk_bf16_f32 v201, v154, v155\n"
      "v_exp_f32_e32 v161, v161\n"
      "v_mfma_f32_16x16x32_bf16 v[172:175], v[224:227], v[116:119], v[172:175]\n"
      "v_cvt_pk_bf16_f32 v194, v156, v157\n"
      "v_cvt_pk_bf16_f32 v195, v158, v159\n"
      "v_mfma_f32_16x16x32_bf16 v[176:179], v[224:227], v[132:135], v[176:179]\n"
      "ds_read_b128 v[224:227], %1 offset:17152\n"
      "v_exp_f32_e32 v162, v162\n"
      "v_mfma_f32_16x16x32_bf16 v[188:191], v[228:231], v[132:135], v[188:191]\n"
      "v_add_f32_e32 v232, v232, v158\n"
      "v_add_f32_e32 v232, v232, v159\n"
      "v_mfma_f32_16x16x32_bf16 v[184:187], v[228:231], v[116:119], v[184:187]\n"
      "v_exp_f32_e32 v163, v163\n"
      "v_mfma_f32_16x16x32_bf16 v[180:183], v[228:231], v[100:103], v[180:183]\n"
      "ds_read_b128 v[228:231], %1 offset:18176\n"
      "v_cvt_pk_bf16_f32 v198, v160, v161\n"
      "s_waitcnt lgkmcnt(2)\n"
      "v_mfma_f32_16x16x32_bf16 v[168:171], v[216:219], v[104:107], v[168:171]\n"
      "v_add_f32_e32 v233, v233, v160\n"
      "v_mfma_f32_16x16x32_bf16 v[172:175], v[216:219], v[120:123], v[172:175]\n"
      "v_exp_f32_e32 v164, v164\n"
      "v_add_f32_e32 v233, v233, v161\n"
      "v_cvt_pk_bf16_f32 v199, v162, v163\n"
      "v_mfma_f32_16x16x32_bf16 v[176:179], v[216:219], v[136:139], v[176:179]\n"
      "ds_read_b64_tr_b16 v[216:217], %2 offset:16384\n"
      "ds_read_b64_tr_b16 v[218:219], %2 offset:17408\n"
      "v_mfma_f32_16x16x32_bf16 v[188:191], v[220:223], v[136:139], v[188:191]\n"
      "v_exp_f32_e32 v165, v165\n"
      "v_mfma_f32_16x16x32_bf16 v[184:187], v[220:223], v[120:123], v[184:187]\n"
      "v_add_f32_e32 v233, v233, v162\n"
      "v_add_f32_e32 v233, v233, v163\n"
      "v_mfma_f32_16x16x32_bf16 v[180:183], v[220:223], v[104:107], v[180:183]\n"
      "ds_read_b64_tr_b16 v[220:221], %3 offset:16384\n"
      "ds_read_b64_tr_b16 v[222:223], %3 offset:17408\n"
      "s_waitcnt lgkmcnt(4)\n"
      "v_mfma_f32_16x16x32_bf16 v[168:171], v[224:227], v[108:111], v[168:171]\n"
      "v_exp_f32_e32 v166, v166\n"
      "v_cvt_pk_bf16_f32 v202, v164, v165\n"
      "v_mfma_f32_16x16x32_bf16 v[172:175], v[224:227], v[124:127], v[172:175]\n"
      "v_add_f32_e32 v234, v234, v164\n"
      "v_exp_f32_e32 v167, v167\n"
      "v_mfma_f32_16x16x32_bf16 v[176:179], v[224:227], v[140:143], v[176:179]\n"
      "ds_read_b64_tr_b16 v[224:225], %2 offset:16640\n"
      "ds_read_b64_tr_b16 v[226:227], %2 offset:17664\n"
      "v_mfma_f32_16x16x32_bf16 v[188:191], v[228:231], v[140:143], v[188:191]\n"
      "v_add_f32_e32 v234, v234, v165\n"
      "v_cvt_pk_bf16_f32 v203, v166, v167\n"
      "v_mfma_f32_16x16x32_bf16 v[184:187], v[228:231], v[124:127], v[184:187]\n"
      "v_add_f32_e32 v234, v234, v166\n"
      "v_add_f32_e32 v234, v234, v167\n"
      "v_mfma_f32_16x16x32_bf16 v[180:183], v[228:231], v[108:111], v[180:183]\n"
      "ds_read_b64_tr_b16 v[228:229], %3 offset:16640\n"
      "ds_read_b64_tr_b16 v[230:231], %3 offset:17664\n"
      "s_waitcnt lgkmcnt(4)\n"
      "v_mfma_f32_16x16x32_bf16 v[0:3], v[216:219], v[192:195], 0\n"
      "v_exp_f32_e32 v168, v168\n"
      "v_mfma_f32_16x16x32_bf16 v[32:35], v[216:219], v[196:199], 0\n"
      "v_exp_f32_e32 v169, v169\n"
      "v_add_f32_e32 v232, v232, v168\n"
      "v_mfma_f32_16x16x32_bf16 v[64:67], v[216:219], v[200:203], 0\n"
      "ds_read_b64_tr_b16 v[216:217], %2 offset:16896\n"
      "ds_read_b64_tr_b16 v[218:219], %2 offset:17920\n"
      "v_mfma_f32_16x16x32_bf16 v[68:71], v[220:223], v[200:203], 0\n"
      "v_add_f32_e32 v232, v232, v169\n"
      "v_exp_f32_e32 v170, v170\n"
      "v_mfma_f32_16x16x32_bf16 v[36:39], v[220:223], v[196:199], 0\n"
      "v_exp_f32_e32 v171, v171\n"
      "v_mfma_f32_16x16x32_bf16 v[4:7], v[220:223], v[192:195], 0\n"
      "ds_read_b64_tr_b16 v[220:221], %3 offset:16896\n"
      "ds_read_b64_tr_b16 v[222:223], %3 offset:17920\n"
      "s_waitcnt lgkmcnt(4)\n"
      "v_mfma_f32_16x16x32_bf16 v[8:11], v[224:227], v[192:195], 0\n"
      "v_add_f32_e32 v232, v232, v170\n"
      "v_add_f32_e32 v232, v232, v171\n"
      "v_mfma_f32_16x16x32_bf16 v[40:43], v[224:227], v[196:199], 0\n"
      "v_exp_f32_e32 v172, v172\n"
      "v_mfma_f32_16x16x32_bf16 v[72:75], v[224:227], v[200:203], 0\n"
      "ds_read_b64_tr_b16 v[224:225], %2 offset:17152\n"
      "ds_read_b64_tr_b16 v[226:227], %2 offset:18176\n"
      "v_mfma_f32_16x16x32_bf16 v[76:79], v[228:231], v[200:203], 0\n"
      "v_exp_f32_e32 v173, v173\n"
      "v_add_f32_e32 v233, v233, v172\n"
      "v_add_f32_e32 v233, v233, v173\n"
      "v_mfma_f32_16x16x32_bf16 v[44:47], v[228:231], v[196:199], 0\n"
      "v_exp_f32_e32 v174, v174\n"
      "v_mfma_f32_16x16x32_bf16 v[12:15], v[228:231], v[192:195], 0\n"
      "ds_read_b64_tr_b16 v[228:229], %3 offset:17152\n"
      "ds_read_b64_tr_b16 v[230:231], %3 offset:18176\n"
      "s_waitcnt lgkmcnt(4)\n"
      "v_mfma_f32_16x16x32_bf16 v[16:19], v[216:219], v[192:195], 0\n"
      "v_exp_f32_e32 v175, v175\n"
      "v_mfma_f32_16x16x32_bf16 v[48:51], v[216:219], v[196:199], 0\n"
      "v_add_f32_e32 v233, v233, v174\n"
      "v_add_f32_e32 v233, v233, v175\n"
      "v_mfma_f32_16x16x32_bf16 v[80:83], v[216:219], v[200:203], 0\n"
      "v_exp_f32_e32 v176, v176\n"
      "v_mfma_f32_16x16x32_bf16 v[84:87], v[220:223], v[200:203], 0\n"
      "s_waitcnt lgkmcnt(0)\n"
      "s_barrier\n"
      "ds_read_b128 v[216:219], %1 offset:32768\n"
      "v_mfma_f32_16x16x32_bf16 v[52:55], v[220:223], v[196:199], 0\n"
      "v_exp_f32_e32 v177, v177\n"
      "v_add_f32_e32 v234, v234, v176\n"
      "v_add_f32_e32 v234, v234, v177\n"
      "v_mfma_f32_16x16x32_bf16 v[20:23], v[220:223], v[192:195], 0\n"
      "ds_read_b128 v[220:223], %1 offset:33792\n"
      "v_mfma_f32_16x16x32_bf16 v[24:27], v[224:227], v[192:195], 0\n"
      "v_exp_f32_e32 v178, v178\n"
      "v_mfma_f32_16x16x32_bf16 v[56:59], v[224:227], v[196:199], 0\n"
      "v_exp_f32_e32 v179, v179\n"
      "v_add_f32_e32 v234, v234, v178\n"
      "v_add_f32_e32 v234, v234, v179\n"
      "v_mfma_f32_16x16x32_bf16 v[88:91], v[224:227], v[200:203], 0\n"
      "ds_read_b128 v[224:227], %1 offset:33024\n"
      "v_mfma_f32_16x16x32_bf16 v[92:95], v[228:231], v[200:203], 0\n"
      "v_exp_f32_e32 v180, v180\n"
      "v_exp_f32_e32 v181, v181\n"
      "v_mfma_f32_16x16x32_bf16 v[60:63], v[228:231], v[196:199], 0\n"
      "v_add_f32_e32 v232, v232, v180\n"
      "v_add_f32_e32 v232, v232, v181\n"
      "v_mfma_f32_16x16x32_bf16 v[28:31], v[228:231], v[192:195], 0\n"
      "ds_read_b128 v[228:231], %1 offset:34048\n"
      "s_branch LT1%=\n"
      "LHEAD%=:\n"
      "s_waitcnt lgkmcnt(2)\n"
      "v_mfma_f32_16x16x32_bf16 v[168:171], v[216:219], v[96:99], v[204:207]\n"
      "v_exp_f32_e32 v158, v158\n"
      "v_mfma_f32_16x16x32_bf16 v[172:175], v[216:219], v[112:115], v[208:211]\n"
      "v_cvt_pk_bf16_f32 v192, v144, v145\n"
      "v_cvt_pk_bf16_f32 v193, v146, v147\n"
      "v_mul_f32_e32 v0, v235, v0\n"
      "v_mul_f32_e32 v1, v235, v1\n"
      "v_mul_f32_e32 v2, v235, v2\n"
      "v_mul_f32_e32 v3, v235, v3\n"
      "v_cvt_pk_bf16_f32 v0, v0, v1\n"
      "v_cvt_pk_bf16_f32 v1, v2, v3\n"
      "v_mfma_f32_16x16x32_bf16 v[176:179], v[216:219], v[128:131], v[212:215]\n"
      "ds_read_b128 v[216:219], %1 offset:16896\n"
      "v_exp_f32_e32 v159, v159\n"
      "v_mul_f32_e32 v32, v236, v32\n"
      "v_mul_f32_e32 v33, v236, v33\n"
      "v_mul_f32_e32 v34, v236, v34\n"
      "v_mul_f32_e32 v35, v236, v35\n"
      "v_cvt_pk_bf16_f32 v32, v32, v33\n"
      "v_mfma_f32_16x16x32_bf16 v[188:191], v[220:223], v[128:131], v[212:215]\n"
      "v_cvt_pk_bf16_f32 v196, v148, v149\n"
      "v_cvt_pk_bf16_f32 v197, v150, v151\n"
      "v_cvt_pk_bf16_f32 v33, v34, v35\n"
      "v_mul_f32_e32 v64, v237, v64\n"
      "v_mul_f32_e32 v65, v237, v65\n"
      "v_mul_f32_e32 v66, v237, v66\n"
      "v_mul_f32_e32 v67, v237, v67\n"
      "v_cvt_pk_bf16_f32 v64, v64, v65\n"
      "v_mfma_f32_16x16x32_bf16 v[184:187], v[220:223], v[112:115], v[208:211]\n"
      "ds_write_b64 %5, v[0:1] offset:0\n"
      "v_exp_f32_e32 v160, v160\n"
      "v_cvt_pk_bf16_f32 v200, v152, v153\n"
      "v_cvt_pk_bf16_f32 v65, v66, v67\n"
      "v_mul_f32_e32 v4, v235, v4\n"
      "v_mul_f32_e32 v5, v235, v5\n"
      "v_mfma_f32_16x16x32_bf16 v[180:183], v[220:223], v[96:99], v[204:207]\n"
      "ds_read_b128 v[220:223], %1 offset:17920\n"
      "v_mul_f32_e32 v6, v235, v6\n"
      "v_mul_f32_e32 v7, v235, v7\n"
      "v_cvt_pk_bf16_f32 v4, v4, v5\n"
      "v_cvt_pk_bf16_f32 v5, v6, v7\n"
      "s_waitcnt lgkmcnt(3)\n"
      "v_mfma_f32_16x16x32_bf16 v[168:171], v[224:227], v[100:103], v[168:171]\n"
      "ds_write_b64 %5, v[32:33] offset:4096\n"
      "v_cvt_pk_bf16_f32 v201, v154, v155\n"
      "v_exp_f32_e32 v161, v161\n"
      "v_mul_f32_e32 v36, v236, v36\n"
      "v_mul_f32_e32 v37, v236, v37\n"
      "v_mul_f32_e32 v38, v236, v38\n"
      "v_mul_f32_e32 v39, v236, v39\n"
      "v_mfma_f32_16x16x32_bf16 v[172:175], v[224:227], v[116:119], v[172:175]\n"
      "ds_write_b64 %5, v[64:65] offset:8192\n"
      "v_cvt_pk_bf16_f32 v194, v156, v157\n"
      "v_cvt_pk_bf16_f32 v195, v158, v159\n"
      "v_cvt_pk_bf16_f32 v36, v36, v37\n"
      "v_cvt_pk_bf16_f32 v37, v38, v39\n"
      "v_mul_f32_e32 v68, v237, v68\n"
      "v_mfma_f32_16x16x32_bf16 v[176:179], v[224:227], v[132:135], v[176:179]\n"
      "ds_read_b128 v[224:227], %1 offset:17152\n"
      "v_exp_f32_e32 v162, v162\n"
      "v_mul_f32_e32 v69, v237, v69\n"
      "v_mul_f32_e32 v70, v237, v70\n"
      "v_mul_f32_e32 v71, v237, v71\n"
      "v_cvt_pk_bf16_f32 v68, v68, v69\n"
      "v_cvt_pk_bf16_f32 v69, v70, v71\n"
      "v_mfma_f32_16x16x32_bf16 v[188:191], v[228:231], v[132:135], v[188:191]\n"
      "ds_write_b64 v238, v[4:5] offset:0\n"
      "v_add_f32_e32 v232, v232, v158\n"
      "v_add_f32_e32 v232, v232, v159\n"
      "v_mul_f32_e32 v8, v235, v8\n"
      "v_mul_f32_e32 v9, v235, v9\n"
      "v_mul_f32_e32 v10, v235, v10\n"
      "v_mfma_f32_16x16x32_bf16 v[184:187], v[228:231], v[116:119], v[184:187]\n"
      "ds_write_b64 v238, v[36:37] offset:4096\n"
      "v_exp_f32_e32 v163, v163\n"
      "v_mul_f32_e32 v11, v235, v11\n"
      "v_cvt_pk_bf16_f32 v8, v8, v9\n"
      "v_cvt_pk_bf16_f32 v9, v10, v11\n"
      "v_mfma_f32_16x16x32_bf16 v[180:183], v[228:231], v[100:103], v[180:183]\n"
      "ds_read_b128 v[228:231], %1 offset:18176\n"
      "v_cvt_pk_bf16_f32 v198, v160, v161\n"
      "v_mul_f32_e32 v40, v236, v40\n"
      "v_mul_f32_e32 v41, v236, v41\n"
      "v_mul_f32_e32 v42, v236, v42\n"
      "v_mul_f32_e32 v43, v236, v43\n"
      "v_cvt_pk_bf16_f32 v40, v40, v41\n"
      "v_cvt_pk_bf16_f32 v41, v42, v43\n"
      "s_waitcnt lgkmcnt(6)\n"
      "v_mfma_f32_16x16x32_bf16 v[168:171], v[216:219], v[104:107], v[168:171]\n"
      "ds_write_b64 v238, v[68:69] offset:8192\n"
      "v_add_f32_e32 v233, v233, v160\n"
      "v_mul_f32_e32 v72, v237, v72\n"
      "v_mul_f32_e32 v73, v237, v73\n"
      "v_mul_f32_e32 v74, v237, v74\n"
      "v_mul_f32_e32 v75, v237, v75\n"
      "v_mfma_f32_16x16x32_bf16 v[172:175], v[216:219], v[120:123], v[172:175]\n"
      "v_exp_f32_e32 v164, v164\n"
      "v_add_f32_e32 v233, v233, v161\n"
      "v_cvt_pk_bf16_f32 v199, v162, v163\n"
      "v_cvt_pk_bf16_f32 v72, v72, v73\n"
      "v_cvt_pk_bf16_f32 v73, v74, v75\n"
      "v_mfma_f32_16x16x32_bf16 v[176:179], v[216:219], v[136:139], v[176:179]\n"
      "ds_read_b64_tr_b16 v[216:217], %2 offset:16384\n"
      "ds_read_b64_tr_b16 v[218:219], %2 offset:17408\n"
      "ds_write_b64 v239, v[8:9] offset:0\n"
      "v_mul_f32_e32 v12, v235, v12\n"
      "v_mul_f32_e32 v13, v235, v13\n"
      "v_mul_f32_e32 v14, v235, v14\n"
      "v_mul_f32_e32 v15, v235, v15\n"
      "v_cvt_pk_bf16_f32 v12, v12, v13\n"
      "v_mfma_f32_16x16x32_bf16 v[188:191], v[220:223], v[136:139], v[188:191]\n"
      "ds_write_b64 v239, v[40:41] offset:4096\n"
      "v_exp_f32_e32 v165, v165\n"
      "v_cvt_pk_bf16_f32 v13, v14, v15\n"
      "v_mul_f32_e32 v44, v236, v44\n"
      "v_mul_f32_e32 v45, v236, v45\n"
      "v_mfma_f32_16x16x32_bf16 v[184:187], v[220:223], v[120:123], v[184:187]\n"
      "v_add_f32_e32 v233, v233, v162\n"
      "v_add_f32_e32 v233, v233, v163\n"
      "v_mul_f32_e32 v46, v236, v46\n"
      "v_mul_f32_e32 v47, v236, v47\n"
      "v_cvt_pk_bf16_f32 v44, v44, v45\n"
      "v_cvt_pk_bf16_f32 v45, v46, v47\n"
      "v_mfma_f32_16x16x32_bf16 v[180:183], v[220:223], v[104:107], v[180:183]\n"
      "ds_read_b64_tr_b16 v[220:221], %3 offset:16384\n"
      "ds_read_b64_tr_b16 v[222:223], %3 offset:17408\n"
      "ds_write_b64 v239, v[72:73] offset:8192\n"
      "v_mul_f32_e32 v76, v237, v76\n"
      "v_mul_f32_e32 v77, v237, v77\n"
      "v_mul_f32_e32 v78, v237, v78\n"
      "v_mul_f32_e32 v79, v237, v79\n"
      "v_cvt_pk_bf16_f32 v76, v76, v77\n"
      "s_waitcnt lgkmcnt(8)\n"
      "v_mfma_f32_16x16x32_bf16 v[168:171], v[224:227], v[108:111], v[168:171]\n"
      "ds_write_b64 v240, v[12:13] offset:0\n"
      "v_exp_f32_e32 v166, v166\n"
      "v_cvt_pk_bf16_f32 v202, v164, v165\n"
      "v_cvt_pk_bf16_f32 v77, v78, v79\n"
      "v_mul_f32_e32 v16, v235, v16\n"
      "v_mfma_f32_16x16x32_bf16 v[172:175], v[224:227], v[124:127], v[172:175]\n"
      "v_add_f32_e32 v234, v234, v164\n"
      "v_exp_f32_e32 v167, v167\n"
      "v_mul_f32_e32 v17, v235, v17\n"
      "v_mul_f32_e32 v18, v235, v18\n"
      "v_mul_f32_e32 v19, v235, v19\n"
      "v_cvt_pk_bf16_f32 v16, v16, v17\n"
      "v_cvt_pk_bf16_f32 v17, v18, v19\n"
      "v_mfma_f32_16x16x32_bf16 v[176:179], v[224:227], v[140:143], v[176:179]\n"
      "ds_read_b64_tr_b16 v[224:225], %2 offset:16640\n"
      "ds_read_b64_tr_b16 v[226:227], %2 offset:17664\n"
      "ds_write_b64 v240, v[44:45] offset:4096\n"
      "v_mul_f32_e32 v48, v236, v48\n"
      "v_mul_f32_e32 v49, v236, v49\n"
      "v_mul_f32_e32 v50, v236, v50\n"
      "v_mfma_f32_16x16x32_bf16 v[188:191], v[228:231], v[140:143], v[188:191]\n"
      "ds_write_b64 v240, v[76:77] offset:8192\n"
      "v_add_f32_e32 v234, v234, v165\n"
      "v_cvt_pk_bf16_f32 v203, v166, v167\n"
      "v_mul_f32_e32 v51, v236, v51\n"
      "v_cvt_pk_bf16_f32 v48, v48, v49\n"
      "v_cvt_pk_bf16_f32 v49, v50, v51\n"
      "v_mfma_f32_16x16x32_bf16 v[184:187], v[228:231], v[124:127], v[184:187]\n"
      "v_add_f32_e32 v234, v234, v166\n"
      "v_add_f32_e32 v234, v234, v167\n"
      "v_mul_f32_e32 v80, v237, v80\n"
      "v_mul_f32_e32 v81, v237, v81\n"
      "v_mul_f32_e32 v82, v237, v82\n"
      "v_mul_f32_e32 v83, v237, v83\n"
      "v_cvt_pk_bf16_f32 v80, v80, v81\n"
      "v_cvt_pk_bf16_f32 v81, v82, v83\n"
      "v_mfma_f32_16x16x32_bf16 v[180:183], v[228:231], v[108:111], v[180:183]\n"
      "ds_read_b64_tr_b16 v[228:229], %3 offset:16640\n"
      "ds_read_b64_tr_b16 v[230:231], %3 offset:17664\n"
      "ds_write_b64 %5, v[16:17] offset:128\n"
      "v_mul_f32_e32 v20, v235, v20\n"
      "v_mul_f32_e32 v21, v235, v21\n"
      "s_waitcnt lgkmcnt(9)\n"
      "v_mfma_f32_16x16x32_bf16 v[0:3], v[216:219], v[192:195], 0\n"
      "ds_write_b64 %5, v[48:49] offset:4224\n"
      "v_exp_f32_e32 v168, v168\n"
      "v_mul_f32_e32 v22, v235, v22\n"
      "v_mul_f32_e32 v23, v235, v23\n"
      "v_cvt_pk_bf16_f32 v20, v20, v21\n"
      "v_cvt_pk_bf16_f32 v21, v22, v23\n"
      "v_mfma_f32_16x16x32_bf16 v[32:35], v[216:219], v[196:199], 0\n"
      "v_exp_f32_e32 v169, v169\n"
      "v_add_f32_e32 v232, v232, v168\n"
      "v_mul_f32_e32 v52, v236, v52\n"
      "v_mul_f32_e32 v53, v236, v53\n"
      "v_mul_f32_e32 v54, v236, v54\n"
      "v_mul_f32_e32 v55, v236, v55\n"
      "v_cvt_pk_bf16_f32 v52, v52, v53\n"
      "v_mfma_f32_16x16x32_bf16 v[64:67], v[216:219], v[200:203], 0\n"
      "ds_read_b64_tr_b16 v[216:217], %2 offset:16896\n"
      "ds_read_b64_tr_b16 v[218:219], %2 offset:17920\n"
      "ds_write_b64 %5, v[80:81] offset:8320\n"
      "v_cvt_pk_bf16_f32 v53, v54, v55\n"
      "v_mul_f32_e32 v84, v237, v84\n"
      "v_mul_f32_e32 v85, v237, v85\n"
      "v_mfma_f32_16x16x32_bf16 v[68:71], v[220:223], v[200:203], 0\n"
      "v_add_f32_e32 v232, v232, v169\n"
      "v_exp_f32_e32 v170, v170\n"
      "v_mul_f32_e32 v86, v237, v86\n"
      "v_mul_f32_e32 v87, v237, v87\n"
      "v_cvt_pk_bf16_f32 v84, v84, v85\n"
      "v_cvt_pk_bf16_f32 v85, v86, v87\n"
      "v_mfma_f32_16x16x32_bf16 v[36:39], v[220:223], v[196:199], 0\n"
      "ds_write_b64 v238, v[20:21] offset:128\n"
      "v_exp_f32_e32 v171, v171\n"
      "v_mul_f32_e32 v24, v235, v24\n"
      "v_mul_f32_e32 v25, v235, v25\n"
      "v_mul_f32_e32 v26, v235, v26\n"
      "v_mul_f32_e32 v27, v235, v27\n"
      "v_mfma_f32_16x16x32_bf16 v[4:7], v[220:223], v[192:195], 0\n"
      "ds_read_b64_tr_b16 v[220:221], %3 offset:16896\n"
      "ds_read_b64_tr_b16 v[222:223], %3 offset:17920\n"
      "ds_write_b64 v238, v[52:53] offset:4224\n"
      "v_cvt_pk_bf16_f32 v24, v24, v25\n"
      "v_cvt_pk_bf16_f32 v25, v26, v27\n"
      "s_waitcnt lgkmcnt(9)\n"
      "v_mfma_f32_16x16x32_bf16 v[8:11], v[224:227], v[192:195], 0\n"
      "v_add_f32_e32 v232, v232, v170\n"
      "v_add_f32_e32 v232, v232, v171\n"
      "v_mul_f32_e32 v56, v236, v56\n"
      "v_mul_f32_e32 v57, v236, v57\n"
      "v_mul_f32_e32 v58, v236, v58\n"
      "v_mul_f32_e32 v59, v236, v59\n"
      "v_cvt_pk_bf16_f32 v56, v56, v57\n"
      "v_cvt_pk_bf16_f32 v57, v58, v59\n"
      "v_mfma_f32_16x16x32_bf16 v[40:43], v[224:227], v[196:199], 0\n"
      "ds_write_b64 v238, v[84:85] offset:8320\n"
      "v_exp_f32_e32 v172, v172\n"
      "v_mul_f32_e32 v88, v237, v88\n"
      "v_mul_f32_e32 v89, v237, v89\n"
      "v_mul_f32_e32 v90, v237, v90\n"
      "v_mul_f32_e32 v91, v237, v91\n"
      "v_mfma_f32_16x16x32_bf16 v[72:75], v[224:227], v[200:203], 0\n"
      "ds_read_b64_tr_b16 v[224:225], %2 offset:17152\n"
      "ds_read_b64_tr_b16 v[226:227], %2 offset:18176\n"
      "ds_write_b64 v239, v[24:25] offset:128\n"
      "v_cvt_pk_bf16_f32 v88, v88, v89\n"
      "v_cvt_pk_bf16_f32 v89, v90, v91\n"
      "v_mul_f32_e32 v28, v235, v28\n"
      "v_mfma_f32_16x16x32_bf16 v[76:79], v[228:231], v[200:203], 0\n"
      "v_exp_f32_e32 v173, v173\n"
      "v_add_f32_e32 v233, v233, v172\n"
      "v_add_f32_e32 v233, v233, v173\n"
      "v_mul_f32_e32 v29, v235, v29\n"
      "v_mul_f32_e32 v30, v235, v30\n"
      "v_mul_f32_e32 v31, v235, v31\n"
      "v_cvt_pk_bf16_f32 v28, v28, v29\n"
      "v_mfma_f32_16x16x32_bf16 v[44:47], v[228:231], v[196:199], 0\n"
      "ds_write_b64 v239, v[56:57] offset:4224\n"
      "v_exp_f32_e32 v174, v174\n"
      "v_cvt_pk_bf16_f32 v29, v30, v31\n"
      "v_mul_f32_e32 v60, v236, v60\n"
      "v_mul_f32_e32 v61, v236, v61\n"
      "v_mfma_f32_16x16x32_bf16 v[12:15], v[228:231], v[192:195], 0\n"
      "ds_read_b64_tr_b16 v[228:229], %3 offset:17152\n"
      "ds_read_b64_tr_b16 v[230:231], %3 offset:18176\n"
      "v_mul_f32_e32 v62, v236, v62\n"
      "v_mul_f32_e32 v63, v236, v63\n"
      "v_cvt_pk_bf16_f32 v60, v60, v61\n"
      "v_cvt_pk_bf16_f32 v61, v62, v63\n"
      "s_waitcnt lgkmcnt(8)\n"
      "v_mfma_f32_16x16x32_bf16 v[16:19], v[216:219], v[192:195], 0\n"
      "ds_write_b64 v239, v[88:89] offset:8320\n"
      "v_exp_f32_e32 v175, v175\n"
      "v_mul_f32_e32 v92, v237, v92\n"
      "v_mul_f32_e32 v93, v237, v93\n"
      "v_mul_f32_e32 v94, v237, v94\n"
      "v_mul_f32_e32 v95, v237, v95\n"
      "v_mfma_f32_16x16x32_bf16 v[48:51], v[216:219], v[196:199], 0\n"
      "ds_write_b64 v240, v[28:29] offset:128\n"
      "v_add_f32_e32 v233, v233, v174\n"
      "v_add_f32_e32 v233, v233, v175\n"
      "v_cvt_pk_bf16_f32 v92, v92, v93\n"
      "v_cvt_pk_bf16_f32 v93, v94, v95\n"
      "v_mfma_f32_16x16x32_bf16 v[80:83], v[216:219], v[200:203], 0\n"
      "ds_write_b64 v240, v[60:61] offset:4224\n"
      "ds_write_b64 v240, v[92:93] offset:8320\n"
      "v_exp_f32_e32 v176, v176\n"
      "v_mfma_f32_16x16x32_bf16 v[84:87], v[220:223], v[200:203], 0\n"
      "s_waitcnt lgkmcnt(0)\n"
      "s_barrier\n"
      "ds_read_b128 v[216:219], %1 offset:32768\n"
      "v_mfma_f32_16x16x32_bf16 v[52:55], v[220:223], v[196:199], 0\n"
      "v_exp_f32_e32 v177, v177\n"
      "v_add_f32_e32 v234, v234, v176\n"
      "v_add_f32_e32 v234, v234, v177\n"
      "v_mfma_f32_16x16x32_bf16 v[20:23], v[220:223], v[192:195], 0\n"
      "ds_read_b128 v[220:223], %1 offset:33792\n"
      "v_mfma_f32_16x16x32_bf16 v[24:27], v[224:227], v[192:195], 0\n"
      "v_exp_f32_e32 v178, v178\n"
      "v_mfma_f32_16x16x32_bf16 v[56:59], v[224:227], v[196:199], 0\n"
      "v_exp_f32_e32 v179, v179\n"
      "v_add_f32_e32 v234, v234, v178\n"
      "v_add_f32_e32 v234, v234, v179\n"
      "v_mfma_f32_16x16x32_bf16 v[88:91], v[224:227], v[200:203], 0\n"
      "ds_read_b128 v[224:227], %1 offset:33024\n"
      "v_mfma_f32_16x16x32_bf16 v[92:95], v[228:231], v[200:203], 0\n"
      "v_exp_f32_e32 v180, v180\n"
      "v_exp_f32_e32 v181, v181\n"
      "v_mfma_f32_16x16x32_bf16 v[60:63], v[228:231], v[196:199], 0\n"
      "v_add_f32_e32 v232, v232, v180\n"
      "v_add_f32_e32 v232, v232, v181\n"
      "v_mfma_f32_16x16x32_bf16 v[28:31], v[228:231], v[192:195], 0\n"
      "ds_read_b128 v[228:231], %1 offset:34048\n"
      "LT1%=:\n"
      "s_waitcnt lgkmcnt(2)\n"
      "v_mfma_f32_16x16x32_bf16 v[144:147], v[216:219], v[96:99], v[204:207]\n"
      "v_exp_f32_e32 v182, v182\n"
      "v_mfma_f32_16x16x32_bf16 v[148:151], v[216:219], v[112:115], v[208:211]\n"
      "v_cvt_pk_bf16_f32 v192, v168, v169\n"
      "v_cvt_pk_bf16_f32 v193, v170, v171\n"
      "v_mfma_f32_16x16x32_bf16 v[152:155], v[216:219], v[128:131], v[212:215]\n"
      "ds_read_b128 v[216:219], %1 offset:33280\n"
      "v_exp_f32_e32 v183, v183\n"
      "v_mfma_f32_16x16x32_bf16 v[164:167], v[220:223], v[128:131], v[212:215]\n"
      "v_cvt_pk_bf16_f32 v196, v172, v173\n"
      "v_cvt_pk_bf16_f32 v197, v174, v175\n"
      "v_mfma_f32_16x16x32_bf16 v[160:163], v[220:223], v[112:115], v[208:211]\n"
      "v_exp_f32_e32 v184, v184\n"
      "v_cvt_pk_bf16_f32 v200, v176, v177\n"
      "v_mfma_f32_16x16x32_bf16 v[156:159], v[220:223], v[96:99], v[204:207]\n"
      "ds_read_b128 v[220:223], %1 offset:34304\n"
      "s_waitcnt lgkmcnt(2)\n"
      "v_mfma_f32_16x16x32_bf16 v[144:147], v[224:227], v[100:103], v[144:147]\n"
      "v_cvt_pk_bf16_f32 v201, v178, v179\n"
      "v_exp_f32_e32 v185, v185\n"
      "v_mfma_f32_16x16x32_bf16 v[148:151], v[224:227], v[116:119], v[148:151]\n"
      "v_cvt_pk_bf16_f32 v194, v180, v181\n"
      "v_cvt_pk_bf16_f32 v195, v182, v183\n"
      "v_mfma_f32_16x16x32_bf16 v[152:155], v[224:227], v[132:135], v[152:155]\n"
      "ds_read_b128 v[224:227], %1 offset:33536\n"
      "v_exp_f32_e32 v186, v186\n"
      "v_mfma_f32_16x16x32_bf16 v[164:167], v[228:231], v[132:135], v[164:167]\n"
      "v_add_f32_e32 v232, v232, v182\n"
      "v_add_f32_e32 v232, v232, v183\n"
      "v_mfma_f32_16x16x32_bf16 v[160:163], v[228:231], v[116:119], v[160:163]\n"
      "v_exp_f32_e32 v187, v187\n"
      "v_mfma_f32_16x16x32_bf16 v[156:159], v[228:231], v[100:103], v[156:159]\n"
      "ds_read_b128 v[228:231], %1 offset:34560\n"
      "v_cvt_pk_bf16_f32 v198, v184, v185\n"
      "s_waitcnt lgkmcnt(2)\n"
      "v_mfma_f32_16x16x32_bf16 v[144:147], v[216:219], v[104:107], v[144:147]\n"
      "v_add_f32_e32 v233, v233, v184\n"
      "v_mfma_f32_16x16x32_bf16 v[148:151], v[216:219], v[120:123], v[148:151]\n"
      "v_exp_f32_e32 v188, v188\n"
      "v_add_f32_e32 v233, v233, v185\n"
      "v_cvt_pk_bf16_f32 v199, v186, v187\n"
      "v_mfma_f32_16x16x32_bf16 v[152:155], v[216:219], v[136:139], v[152:155]\n"
      "ds_read_b64_tr_b16 v[216:217], %2 offset:32768\n"
      "ds_read_b64_tr_b16 v[218:219], %2 offset:33792\n"
      "v_mfma_f32_16x16x32_bf16 v[164:167], v[220:223], v[136:139], v[164:167]\n"
      "v_exp_f32_e32 v189, v189\n"
      "v_mfma_f32_16x16x32_bf16 v[160:163], v[220:223], v[120:123], v[160:163]\n"
      "v_add_f32_e32 v233, v233, v186\n"
      "v_add_f32_e32 v233, v233, v187\n"
      "v_mfma_f32_16x16x32_bf16 v[156:159], v[220:223], v[104:107], v[156:159]\n"
      "ds_read_b64_tr_b16 v[220:221], %3 offset:32768\n"
      "ds_read_b64_tr_b16 v[222:223], %3 offset:33792\n"
      "s_waitcnt lgkmcnt(4)\n"
      "v_mfma_f32_16x16x32_bf16 v[144:147], v[224:227], v[108:111], v[144:147]\n"
      "v_exp_f32_e32 v190, v190\n"
      "v_cvt_pk_bf16_f32 v202, v188, v189\n"
      "v_mfma_f32_16x16x32_bf16 v[148:151], v[224:227], v[124:127], v[148:151]\n"
      "v_add_f32_e32 v234, v234, v188\n"
      "v_exp_f32_e32 v191, v191\n"
      "v_mfma_f32_16x16x32_bf16 v[152:155], v[224:227], v[140:143], v[152:155]\n"
      "ds_read_b64_tr_b16 v[224:225], %2 offset:33024\n"
      "ds_read_b64_tr_b16 v[226:227], %2 offset:34048\n"
      "v_mfma_f32_16x16x32_bf16 v[164:167], v[228:231], v[140:143], v[164:167]\n"
      "v_add_f32_e32 v234, v234, v189\n"
      "v_cvt_pk_bf16_f32 v203, v190, v191\n"
      "v_mfma_f32_16x16x32_bf16 v[160:163], v[228:231], v[124:127], v[160:163]\n"
      "v_add_f32_e32 v234, v234, v190\n"
      "v_add_f32_e32 v234, v234, v191\n"
      "v_mfma_f32_16x16x32_bf16 v[156:159], v[228:231], v[108:111], v[156:159]\n"
      "ds_read_b64_tr_b16 v[228:229], %3 offset:33024\n"
      "ds_read_b64_tr_b16 v[230:231], %3 offset:34048\n"
      "s_waitcnt lgkmcnt(4)\n"
      "v_mfma_f32_16x16x32_bf16 v[0:3], v[216:219], v[192:195], v[0:3]\n"
      "v_exp_f32_e32 v144, v144\n"
      "v_mfma_f32_16x16x32_bf16 v[32:35], v[216:219], v[196:199], v[32:35]\n"
      "v_exp_f32_e32 v145, v145\n"
      "v_add_f32_e32 v232, v232, v144\n"
      "v_mfma_f32_16x16x32_bf16 v[64:67], v[216:219], v[200:203], v[64:67]\n"
      "ds_read_b64_tr_b16 v[216:217], %2 offset:33280\n"
      "ds_read_b64_tr_b16 v[218:219], %2 offset:34304\n"
      "v_mfma_f32_16x16x32_bf16 v[68:71], v[220:223], v[200:203], v[68:71]\n"
      "v_add_f32_e32 v232, v232, v145\n"
      "v_exp_f32_e32 v146, v146\n"
      "v_mfma_f32_16x16x32_bf16 v[36:39], v[220:223], v[196:199], v[36:39]\n"
      "v_exp_f32_e32 v147, v147\n"
      "v_mfma_f32_16x16x32_bf16 v[4:7], v[220:223], v[192:195], v[4:7]\n"
      "ds_read_b64_tr_b16 v[220:221], %3 offset:33280\n"
      "ds_read_b64_tr_b16 v[222:223], %3 offset:34304\n"
      "s_waitcnt lgkmcnt(4)\n"
      "v_mfma_f32_16x16x32_bf16 v[8:11], v[224:227], v[192:195], v[8:11]\n"
      "v_add_f32_e32 v232, v232, v146\n"
      "v_add_f32_e32 v232, v232, v147\n"
      "v_mfma_f32_16x16x32_bf16 v[40:43], v[224:227], v[196:199], v[40:43]\n"
      "v_exp_f32_e32 v148, v148\n"
      "v_mfma_f32_16x16x32_bf16 v[72:75], v[224:227], v[200:203], v[72:75]\n"
      "ds_read_b64_tr_b16 v[224:225], %2 offset:33536\n"
      "ds_read_b64_tr_b16 v[226:227], %2 offset:34560\n"
      "v_mfma_f32_16x16x32_bf16 v[76:79], v[228:231], v[200:203], v[76:79]\n"
      "v_exp_f32_e32 v149, v149\n"
      "v_add_f32_e32 v233, v233, v148\n"
      "v_add_f32_e32 v233, v233, v149\n"
      "v_mfma_f32_16x16x32_bf16 v[44:47], v[228:231], v[196:199], v[44:47]\n"
      "v_exp_f32_e32 v150, v150\n"
      "v_mfma_f32_16x16x32_bf16 v[12:15], v[228:231], v[192:195], v[12:15]\n"
      "ds_read_b64_tr_b16 v[228:229], %3 offset:33536\n"
      "ds_read_b64_tr_b16 v[230:231], %3 offset:34560\n"
      "s_waitcnt lgkmcnt(4)\n"
      "v_mfma_f32_16x16x32_bf16 v[16:19], v[216:219], v[192:195], v[16:19]\n"
      "v_exp_f32_e32 v151, v151\n"
      "v_mfma_f32_16x16x32_bf16 v[48:51], v[216:219], v[196:199], v[48:51]\n"
      "v_add_f32_e32 v233, v233, v150\n"
      "v_add_f32_e32 v233, v233, v151\n"
      "v_mfma_f32_16x16x32_bf16 v[80:83], v[216:219], v[200:203], v[80:83]\n"
      "v_exp_f32_e32 v152, v152\n"
      "v_mfma_f32_16x16x32_bf16 v[84:87], v[220:223], v[200:203], v[84:87]\n"
      "s_waitcnt lgkmcnt(0)\n"
      "s_barrier\n"
      "ds_read_b128 v[216:219], %1 offset:49152\n"
      "v_mfma_f32_16x16x32_bf16 v[52:55], v[220:223], v[196:199], v[52:55]\n"
      "v_exp_f32_e32 v153, v153\n"
      "v_add_f32_e32 v234, v234, v152\n"
      "v_add_f32_e32 v234, v234, v153\n"
      "v_mfma_f32_16x16x32_bf16 v[20:23], v[220:223], v[192:195], v[20:23]\n"
      "ds_read_b128 v[220:223], %1 offset:50176\n"
      "v_mfma_f32_16x16x32_bf16 v[24:27], v[224:227], v[192:195], v[24:27]\n"
      "v_exp_f32_e32 v154, v154\n"
      "v_mfma_f32_16x16x32_bf16 v[56:59], v[224:227], v[196:199], v[56:59]\n"
      "v_exp_f32_e32 v155, v155\n"
      "v_add_f32_e32 v234, v234, v154\n"
      "v_add_f32_e32 v234, v234, v155\n"
      "v_mfma_f32_16x16x32_bf16 v[88:91], v[224:227], v[200:203], v[88:91]\n"
      "ds_read_b128 v[224:227], %1 offset:49408\n"
      "v_mfma_f32_16x16x32_bf16 v[92:95], v[228:231], v[200:203], v[92:95]\n"
      "v_exp_f32_e32 v156, v156\n"
      "v_exp_f32_e32 v157, v157\n"
      "v_mfma_f32_16x16x32_bf16 v[60:63], v[228:231], v[196:199], v[60:63]\n"
      "v_add_f32_e32 v232, v232, v156\n"
      "v_add_f32_e32 v232, v232, v157\n"
      "v_mfma_f32_16x16x32_bf16 v[28:31], v[228:231], v[192:195], v[28:31]\n"
      "ds_read_b128 v[228:231], %1 offset:50432\n"
      "s_waitcnt lgkmcnt(2)\n"
      "v_mfma_f32_16x16x32_bf16 v[168:171], v[216:219], v[96:99], v[204:207]\n"
      "v_exp_f32_e32 v158, v158\n"
      "v_mfma_f32_16x16x32_bf16 v[172:175], v[216:219], v[112:115], v[208:211]\n"
      "v_cvt_pk_bf16_f32 v192, v144, v145\n"
      "v_cvt_pk_bf16_f32 v193, v146, v147\n"
      "v_mfma_f32_16x16x32_bf16 v[176:179], v[216:219], v[128:131], v[212:215]\n"
      "ds_read_b128 v[216:219], %1 offset:49664\n"
      "v_exp_f32_e32 v159, v159\n"
      "v_mfma_f32_16x16x32_bf16 v[188:191], v[220:223], v[128:131], v[212:215]\n"
      "v_cvt_pk_bf16_f32 v196, v148, v149\n"
      "v_cvt_pk_bf16_f32 v197, v150, v151\n"
      "v_mfma_f32_16x16x32_bf16 v[184:187], v[220:223], v[112:115], v[208:211]\n"
      "v_exp_f32_e32 v160, v160\n"
      "v_cvt_pk_bf16_f32 v200, v152, v153\n"
      "v_mfma_f32_16x16x32_bf16 v[180:183], v[220:223], v[96:99], v[204:207]\n"
      "ds_read_b128 v[220:223], %1 offset:50688\n"
      "s_waitcnt lgkmcnt(2)\n"
      "v_mfma_f32_16x16x32_bf16 v[168:171], v[224:227], v[100:103], v[168:171]\n"
      "v_cvt_pk_bf16_f32 v201, v154, v155\n"
      "v_exp_f32_e32 v161, v161\n"
      "v_mfma_f32_16x16x32_bf16 v[172:175], v[224:227], v[116:119], v[172:175]\n"
      "v_cvt_pk_bf16_f32 v194, v156, v157\n"
      "v_cvt_pk_bf16_f32 v195, v158, v159\n"
      "v_mfma_f32_16x16x32_bf16 v[176:179], v[224:227], v[132:135], v[176:179]\n"
      "ds_read_b128 v[224:227], %1 offset:49920\n"
      "v_exp_f32_e32 v162, v162\n"
      "v_mfma_f32_16x16x32_bf16 v[188:191], v[228:231], v[132:135], v[188:191]\n"
      "v_add_f32_e32 v232, v232, v158\n"
      "v_add_f32_e32 v232, v232, v159\n"
      "v_mfma_f32_16x16x32_bf16 v[184:187], v[228:231], v[116:119], v[184:187]\n"
      "v_exp_f32_e32 v163, v163\n"
      "v_mfma_f32_16x16x32_bf16 v[180:183], v[228:231], v[100:103], v[180:183]\n"
      "ds_read_b128 v[228:231], %1 offset:50944\n"
      "v_cvt_pk_bf16_f32 v198, v160, v161\n"
      "s_waitcnt lgkmcnt(2)\n"
      "v_mfma_f32_16x16x32_bf16 v[168:171], v[216:219], v[104:107], v[168:171]\n"
      "v_add_f32_e32 v233, v233, v160\n"
      "v_mfma_f32_16x16x32_bf16 v[172:175], v[216:219], v[120:123], v[172:175]\n"
      "v_exp_f32_e32 v164, v164\n"
      "v_add_f32_e32 v233, v233, v161\n"
      "v_cvt_pk_bf16_f32 v199, v162, v163\n"
      "v_mfma_f32_16x16x32_bf16 v[176:179], v[216:219], v[136:139], v[176:179]\n"
      "ds_read_b64_tr_b16 v[216:217], %2 offset:49152\n"
      "ds_read_b64_tr_b16 v[218:219], %2 offset:50176\n"
      "v_mfma_f32_16x16x32_bf16 v[188:191], v[220:223], v[136:139], v[188:191]\n"
      "v_exp_f32_e32 v165, v165\n"
      "v_mfma_f32_16x16x32_bf16 v[184:187], v[220:223], v[120:123], v[184:187]\n"
      "v_add_f32_e32 v233, v233, v162\n"
      "v_add_f32_e32 v233, v233, v163\n"
      "v_mfma_f32_16x16x32_bf16 v[180:183], v[220:223], v[104:107], v[180:183]\n"
      "ds_read_b64_tr_b16 v[220:221], %3 offset:49152\n"
      "ds_read_b64_tr_b16 v[222:223], %3 offset:50176\n"
      "s_waitcnt lgkmcnt(4)\n"
      "v_mfma_f32_16x16x32_bf16 v[168:171], v[224:227], v[108:111], v[168:171]\n"
      "v_exp_f32_e32 v166, v166\n"
      "v_cvt_pk_bf16_f32 v202, v164, v165\n"
      "v_mfma_f32_16x16x32_bf16 v[172:175], v[224:227], v[124:127], v[172:175]\n"
      "v_add_f32_e32 v234, v234, v164\n"
      "v_exp_f32_e32 v167, v167\n"
      "v_mfma_f32_16x16x32_bf16 v[176:179], v[224:227], v[140:143], v[176:179]\n"
      "ds_read_b64_tr_b16 v[224:225], %2 offset:49408\n"
      "ds_read_b64_tr_b16 v[226:227], %2 offset:50432\n"
      "v_mfma_f32_16x16x32_bf16 v[188:191], v[228:231], v[140:143], v[188:191]\n"
      "v_add_f32_e32 v234, v234, v165\n"
      "v_cvt_pk_bf16_f32 v203, v166, v167\n"
      "v_mfma_f32_16x16x32_bf16 v[184:187], v[228:231], v[124:127], v[184:187]\n"
      "v_add_f32_e32 v234, v234, v166\n"
      "v_add_f32_e32 v234, v234, v167\n"
      "v_mfma_f32_16x16x32_bf16 v[180:183], v[228:231], v[108:111], v[180:183]\n"
      "ds_read_b64_tr_b16 v[228:229], %3 offset:49408\n"
      "ds_read_b64_tr_b16 v[230:231], %3 offset:50432\n"
      "s_waitcnt lgkmcnt(4)\n"
      "v_mfma_f32_16x16x32_bf16 v[0:3], v[216:219], v[192:195], v[0:3]\n"
      "v_exp_f32_e32 v168, v168\n"
      "v_mfma_f32_16x16x32_bf16 v[32:35], v[216:219], v[196:199], v[32:35]\n"
      "v_exp_f32_e32 v169, v169\n"
      "v_add_f32_e32 v232, v232, v168\n"
      "v_mfma_f32_16x16x32_bf16 v[64:67], v[216:219], v[200:203], v[64:67]\n"
      "ds_read_b64_tr_b16 v[216:217], %2 offset:49664\n"
      "ds_read_b64_tr_b16 v[218:219], %2 offset:50688\n"
      "v_mfma_f32_16x16x32_bf16 v[68:71], v[220:223], v[200:203], v[68:71]\n"
      "v_add_f32_e32 v232, v232, v169\n"
      "v_exp_f32_e32 v170, v170\n"
      "v_mfma_f32_16x16x32_bf16 v[36:39], v[220:223], v[196:199], v[36:39]\n"
      "v_exp_f32_e32 v171, v171\n"
      "v_mfma_f32_16x16x32_bf16 v[4:7], v[220:223], v[192:195], v[4:7]\n"
      "ds_read_b64_tr_b16 v[220:221], %3 offset:49664\n"
      "ds_read_b64_tr_b16 v[222:223], %3 offset:50688\n"
      "s_waitcnt lgkmcnt(4)\n"
      "v_mfma_f32_16x16x32_bf16 v[8:11], v[224:227], v[192:195], v[8:11]\n"
      "v_add_f32_e32 v232, v232, v170\n"
      "v_add_f32_e32 v232, v232, v171\n"
      "v_mfma_f32_16x16x32_bf16 v[40:43], v[224:227], v[196:199], v[40:43]\n"
      "v_exp_f32_e32 v172, v172\n"
      "v_mfma_f32_16x16x32_bf16 v[72:75], v[224:227], v[200:203], v[72:75]\n"
      "ds_read_b64_tr_b16 v[224:225], %2 offset:49920\n"
      "ds_read_b64_tr_b16 v[226:227], %2 offset:50944\n"
      "v_mfma_f32_16x16x32_bf16 v[76:79], v[228:231], v[200:203], v[76:79]\n"
      "v_exp_f32_e32 v173, v173\n"
      "v_add_f32_e32 v233, v233, v172\n"
      "v_add_f32_e32 v233, v233, v173\n"
      "v_mfma_f32_16x16x32_bf16 v[44:47], v[228:231], v[196:199], v[44:47]\n"
      "v_exp_f32_e32 v174, v174\n"
      "v_mfma_f32_16x16x32_bf16 v[12:15], v[228:231], v[192:195], v[12:15]\n"
      "ds_read_b64_tr_b16 v[228:229], %3 offset:49920\n"
      "ds_read_b64_tr_b16 v[230:231], %3 offset:50944\n"
      "s_waitcnt lgkmcnt(4)\n"
      "v_mfma_f32_16x16x32_bf16 v[16:19], v[216:219], v[192:195], v[16:19]\n"
      "v_exp_f32_e32 v175, v175\n"
      "v_mfma_f32_16x16x32_bf16 v[48:51], v[216:219], v[196:199], v[48:51]\n"
      "v_add_f32_e32 v233, v233, v174\n"
      "v_add_f32_e32 v233, v233, v175\n"
      "v_mfma_f32_16x16x32_bf16 v[80:83], v[216:219], v[200:203], v[80:83]\n"
      "v_exp_f32_e32 v176, v176\n"
      "v_mfma_f32_16x16x32_bf16 v[84:87], v[220:223], v[200:203], v[84:87]\n"
      "s_waitcnt lgkmcnt(0)\n"
      "s_barrier\n"
      "ds_read_b128 v[216:219], %1 offset:0\n"
      "v_mfma_f32_16x16x32_bf16 v[52:55], v[220:223], v[196:199], v[52:55]\n"
      "v_exp_f32_e32 v177, v177\n"
      "v_add_f32_e32 v234, v234, v176\n"
      "v_add_f32_e32 v234, v234, v177\n"
      "v_mfma_f32_16x16x32_bf16 v[20:23], v[220:223], v[192:195], v[20:23]\n"
      "ds_read_b128 v[220:223], %1 offset:1024\n"
      "v_mfma_f32_16x16x32_bf16 v[24:27], v[224:227], v[192:195], v[24:27]\n"
      "v_exp_f32_e32 v178, v178\n"
      "v_mfma_f32_16x16x32_bf16 v[56:59], v[224:227], v[196:199], v[56:59]\n"
      "v_exp_f32_e32 v179, v179\n"
      "v_add_f32_e32 v234, v234, v178\n"
      "v_add_f32_e32 v234, v234, v179\n"
      "v_mfma_f32_16x16x32_bf16 v[88:91], v[224:227], v[200:203], v[88:91]\n"
      "ds_read_b128 v[224:227], %1 offset:256\n"
      "v_mfma_f32_16x16x32_bf16 v[92:95], v[228:231], v[200:203], v[92:95]\n"
      "v_exp_f32_e32 v180, v180\n"
      "v_exp_f32_e32 v181, v181\n"
      "v_mfma_f32_16x16x32_bf16 v[60:63], v[228:231], v[196:199], v[60:63]\n"
      "v_add_f32_e32 v232, v232, v180\n"
      "v_add_f32_e32 v232, v232, v181\n"
      "v_mfma_f32_16x16x32_bf16 v[28:31], v[228:231], v[192:195], v[28:31]\n"
      "ds_read_b128 v[228:231], %1 offset:1280\n"
      "s_waitcnt lgkmcnt(2)\n"
      "v_mfma_f32_16x16x32_bf16 v[144:147], v[216:219], v[96:99], v[204:207]\n"
      "v_exp_f32_e32 v182, v182\n"
      "v_mfma_f32_16x16x32_bf16 v[148:151], v[216:219], v[112:115], v[208:211]\n"
      "v_cvt_pk_bf16_f32 v192, v168, v169\n"
      "v_cvt_pk_bf16_f32 v193, v170, v171\n"
      "v_mfma_f32_16x16x32_bf16 v[152:155], v[216:219], v[128:131], v[212:215]\n"
      "ds_read_b128 v[216:219], %1 offset:512\n"
      "v_exp_f32_e32 v183, v183\n"
      "v_mfma_f32_16x16x32_bf16 v[164:167], v[220:223], v[128:131], v[212:215]\n"
      "v_cvt_pk_bf16_f32 v196, v172, v173\n"
      "v_cvt_pk_bf16_f32 v197, v174, v175\n"
      "v_mfma_f32_16x16x32_bf16 v[160:163], v[220:223], v[112:115], v[208:211]\n"
      "v_exp_f32_e32 v184, v184\n"
      "v_cvt_pk_bf16_f32 v200, v176, v177\n"
      "v_mfma_f32_16x16x32_bf16 v[156:159], v[220:223], v[96:99], v[204:207]\n"
      "ds_read_b128 v[220:223], %1 offset:1536\n"
      "s_waitcnt lgkmcnt(2)\n"
      "v_mfma_f32_16x16x32_bf16 v[144:147], v[224:227], v[100:103], v[144:147]\n"
      "v_cvt_pk_bf16_f32 v201, v178, v179\n"
      "v_exp_f32_e32 v185, v185\n"
      "v_mfma_f32_16x16x32_bf16 v[148:151], v[224:227], v[116:119], v[148:151]\n"
      "v_cvt_pk_bf16_f32 v194, v180, v181\n"
      "v_cvt_pk_bf16_f32 v195, v182, v183\n"
      "v_mfma_f32_16x16x32_bf16 v[152:155], v[224:227], v[132:135], v[152:155]\n"
      "ds_read_b128 v[224:227], %1 offset:768\n"
      "v_exp_f32_e32 v186, v186\n"
      "v_mfma_f32_16x16x32_bf16 v[164:167], v[228:231], v[132:135], v[164:167]\n"
      "v_add_f32_e32 v232, v232, v182\n"
      "v_add_f32_e32 v232, v232, v183\n"
      "v_mfma_f32_16x16x32_bf16 v[160:163], v[228:231], v[116:119], v[160:163]\n"
      "v_exp_f32_e32 v187, v187\n"
      "v_mfma_f32_16x16x32_bf16 v[156:159], v[228:231], v[100:103], v[156:159]\n"
      "ds_read_b128 v[228:231], %1 offset:1792\n"
      "v_cvt_pk_bf16_f32 v198, v184, v185\n"
      "s_waitcnt lgkmcnt(2)\n"
      "v_mfma_f32_16x16x32_bf16 v[144:147], v[216:219], v[104:107], v[144:147]\n"
      "v_add_f32_e32 v233, v233, v184\n"
      "v_mfma_f32_16x16x32_bf16 v[148:151], v[216:219], v[120:123], v[148:151]\n"
      "v_exp_f32_e32 v188, v188\n"
      "v_add_f32_e32 v233, v233, v185\n"
      "v_cvt_pk_bf16_f32 v199, v186, v187\n"
      "v_mfma_f32_16x16x32_bf16 v[152:155], v[216:219], v[136:139], v[152:155]\n"
      "ds_read_b64_tr_b16 v[216:217], %2 offset:0\n"
      "ds_read_b64_tr_b16 v[218:219], %2 offset:1024\n"
      "v_mfma_f32_16x16x32_bf16 v[164:167], v[220:223], v[136:139], v[164:167]\n"
      "v_exp_f32_e32 v189, v189\n"
      "v_mfma_f32_16x16x32_bf16 v[160:163], v[220:223], v[120:123], v[160:163]\n"
      "v_add_f32_e32 v233, v233, v186\n"
      "v_add_f32_e32 v233, v233, v187\n"
      "v_mfma_f32_16x16x32_bf16 v[156:159], v[220:223], v[104:107], v[156:159]\n"
      "ds_read_b64_tr_b16 v[220:221], %3 offset:0\n"
      "ds_read_b64_tr_b16 v[222:223], %3 offset:1024\n"
      "s_waitcnt lgkmcnt(4)\n"
      "v_mfma_f32_16x16x32_bf16 v[144:147], v[224:227], v[108:111], v[144:147]\n"
      "v_exp_f32_e32 v190, v190\n"
      "v_cvt_pk_bf16_f32 v202, v188, v189\n"
      "v_mfma_f32_16x16x32_bf16 v[148:151], v[224:227], v[124:127], v[148:151]\n"
      "v_add_f32_e32 v234, v234, v188\n"
      "v_exp_f32_e32 v191, v191\n"
      "v_mfma_f32_16x16x32_bf16 v[152:155], v[224:227], v[140:143], v[152:155]\n"
      "ds_read_b64_tr_b16 v[224:225], %2 offset:256\n"
      "ds_read_b64_tr_b16 v[226:227], %2 offset:1280\n"
      "v_mfma_f32_16x16x32_bf16 v[164:167], v[228:231], v[140:143], v[164:167]\n"
      "v_add_f32_e32 v234, v234, v189\n"
      "v_cvt_pk_bf16_f32 v203, v190, v191\n"
      "v_mfma_f32_16x16x32_bf16 v[160:163], v[228:231], v[124:127], v[160:163]\n"
      "v_add_f32_e32 v234, v234, v190\n"
      "v_add_f32_e32 v234, v234, v191\n"
      "v_mfma_f32_16x16x32_bf16 v[156:159], v[228:231], v[108:111], v[156:159]\n"
      "ds_read_b64_tr_b16 v[228:229], %3 offset:256\n"
      "ds_read_b64_tr_b16 v[230:231], %3 offset:1280\n"
      "s_waitcnt lgkmcnt(4)\n"
      "v_mfma_f32_16x16x32_bf16 v[0:3], v[216:219], v[192:195], v[0:3]\n"
      "v_exp_f32_e32 v144, v144\n"
      "v_mfma_f32_16x16x32_bf16 v[32:35], v[216:219], v[196:199], v[32:35]\n"
      "v_exp_f32_e32 v145, v145\n"
      "v_add_f32_e32 v232, v232, v144\n"
      "v_mfma_f32_16x16x32_bf16 v[64:67], v[216:219], v[200:203], v[64:67]\n"
      "ds_read_b64_tr_b16 v[216:217], %2 offset:512\n"
      "ds_read_b64_tr_b16 v[218:219], %2 offset:1536\n"
      "v_mfma_f32_16x16x32_bf16 v[68:71], v[220:223], v[200:203], v[68:71]\n"
      "v_add_f32_e32 v232, v232, v145\n"
      "v_exp_f32_e32 v146, v146\n"
      "v_mfma_f32_16x16x32_bf16 v[36:39], v[220:223], v[196:199], v[36:39]\n"
      "v_exp_f32_e32 v147, v147\n"
      "v_mfma_f32_16x16x32_bf16 v[4:7], v[220:223], v[192:195], v[4:7]\n"
      "ds_read_b64_tr_b16 v[220:221], %3 offset:512\n"
      "ds_read_b64_tr_b16 v[222:223], %3 offset:1536\n"
      "s_waitcnt lgkmcnt(4)\n"
      "v_mfma_f32_16x16x32_bf16 v[8:11], v[224:227], v[192:195], v[8:11]\n"
      "v_add_f32_e32 v232, v232, v146\n"
      "v_add_f32_e32 v232, v232, v147\n"
      "v_mfma_f32_16x16x32_bf16 v[40:43], v[224:227], v[196:199], v[40:43]\n"
      "v_exp_f32_e32 v148, v148\n"
      "v_mfma_f32_16x16x32_bf16 v[72:75], v[224:227], v[200:203], v[72:75]\n"
      "ds_read_b64_tr_b16 v[224:225], %2 offset:768\n"
      "ds_read_b64_tr_b16 v[226:227], %2 offset:1792\n"
      "v_mfma_f32_16x16x32_bf16 v[76:79], v[228:231], v[200:203], v[76:79]\n"
      "v_exp_f32_e32 v149, v149\n"
      "v_add_f32_e32 v233, v233, v148\n"
      "v_add_f32_e32 v233, v233, v149\n"
      "v_mfma_f32_16x16x32_bf16 v[44:47], v[228:231], v[196:199], v[44:47]\n"
      "v_exp_f32_e32 v150, v150\n"
      "v_mfma_f32_16x16x32_bf16 v[12:15], v[228:231], v[192:195], v[12:15]\n"
      "ds_read_b64_tr_b16 v[228:229], %3 offset:768\n"
      "ds_read_b64_tr_b16 v[230:231], %3 offset:1792\n"
      "s_waitcnt lgkmcnt(4)\n"
      "v_mfma_f32_16x16x32_bf16 v[16:19], v[216:219], v[192:195], v[16:19]\n"
      "v_exp_f32_e32 v151, v151\n"
      "v_mfma_f32_16x16x32_bf16 v[48:51], v[216:219], v[196:199], v[48:51]\n"
      "v_add_f32_e32 v233, v233, v150\n"
      "v_add_f32_e32 v233, v233, v151\n"
      "v_mfma_f32_16x16x32_bf16 v[80:83], v[216:219], v[200:203], v[80:83]\n"
      "v_exp_f32_e32 v152, v152\n"
      "v_mfma_f32_16x16x32_bf16 v[84:87], v[220:223], v[200:203], v[84:87]\n"
      "s_waitcnt lgkmcnt(0)\n"
      "s_barrier\n"
      "ds_read_b128 v[216:219], %1 offset:16384\n"
      "v_mfma_f32_16x16x32_bf16 v[52:55], v[220:223], v[196:199], v[52:55]\n"
      "v_exp_f32_e32 v153, v153\n"
      "v_add_f32_e32 v234, v234, v152\n"
      "v_add_f32_e32 v234, v234, v153\n"
      "v_mfma_f32_16x16x32_bf16 v[20:23], v[220:223], v[192:195], v[20:23]\n"
      "ds_read_b128 v[220:223], %1 offset:17408\n"
      "v_mfma_f32_16x16x32_bf16 v[24:27], v[224:227], v[192:195], v[24:27]\n"
      "v_exp_f32_e32 v154, v154\n"
      "v_mfma_f32_16x16x32_bf16 v[56:59], v[224:227], v[196:199], v[56:59]\n"
      "v_exp_f32_e32 v155, v155\n"
      "v_add_f32_e32 v234, v234, v154\n"
      "v_add_f32_e32 v234, v234, v155\n"
      "v_mfma_f32_16x16x32_bf16 v[88:91], v[224:227], v[200:203], v[88:91]\n"
      "ds_read_b128 v[224:227], %1 offset:16640\n"
      "v_mfma_f32_16x16x32_bf16 v[92:95], v[228:231], v[200:203], v[92:95]\n"
      "v_exp_f32_e32 v156, v156\n"
      "v_exp_f32_e32 v157, v157\n"
      "v_mfma_f32_16x16x32_bf16 v[60:63], v[228:231], v[196:199], v[60:63]\n"
      "v_add_f32_e32 v232, v232, v156\n"
      "v_add_f32_e32 v232, v232, v157\n"
      "v_mfma_f32_16x16x32_bf16 v[28:31], v[228:231], v[192:195], v[28:31]\n"
      "ds_read_b128 v[228:231], %1 offset:17664\n"
      "s_mov_b32 s41, 2\n"
      "LLOOP%=:\n"
      "s_waitcnt lgkmcnt(2)\n"
      "v_mfma_f32_16x16x32_bf16 v[168:171], v[216:219], v[96:99], v[204:207]\n"
      "v_exp_f32_e32 v158, v158\n"
      "v_mfma_f32_16x16x32_bf16 v[172:175], v[216:219], v[112:115], v[208:211]\n"
      "v_cvt_pk_bf16_f32 v192, v144, v145\n"
      "v_cvt_pk_bf16_f32 v193, v146, v147\n"
      "v_mfma_f32_16x16x32_bf16 v[176:179], v[216:219], v[128:131], v[212:215]\n"
      "ds_read_b128 v[216:219], %1 offset:16896\n"
      "v_exp_f32_e32 v159, v159\n"
      "v_mfma_f32_16x16x32_bf16 v[188:191], v[220:223], v[128:131], v[212:215]\n"
      "v_cvt_pk_bf16_f32 v196, v148, v149\n"
      "v_cvt_pk_bf16_f32 v197, v150, v151\n"
      "v_mfma_f32_16x16x32_bf16 v[184:187], v[220:223], v[112:115], v[208:211]\n"
      "v_exp_f32_e32 v160, v160\n"
      "v_cvt_pk_bf16_f32 v200, v152, v153\n"
      "v_mfma_f32_16x16x32_bf16 v[180:183], v[220:223], v[96:99], v[204:207]\n"
      "ds_read_b128 v[220:223], %1 offset:17920\n"
      "s_waitcnt lgkmcnt(2)\n"
      "v_mfma_f32_16x16x32_bf16 v[168:171], v[224:227], v[100:103], v[168:171]\n"
      "v_cvt_pk_bf16_f32 v201, v154, v155\n"
      "v_exp_f32_e32 v161, v161\n"
      "v_mfma_f32_16x16x32_bf16 v[172:175], v[224:227], v[116:119], v[172:175]\n"
      "v_cvt_pk_bf16_f32 v194, v156, v157\n"
      "v_cvt_pk_bf16_f32 v195, v158, v159\n"
      "v_mfma_f32_16x16x32_bf16 v[176:179], v[224:227], v[132:135], v[176:179]\n"
      "ds_read_b128 v[224:227], %1 offset:17152\n"
      "v_exp_f32_e32 v162, v162\n"
      "v_mfma_f32_16x16x32_bf16 v[188:191], v[228:231], v[132:135], v[188:191]\n"
      "v_add_f32_e32 v232, v232, v158\n"
      "v_add_f32_e32 v232, v232, v159\n"
      "v_mfma_f32_16x16x32_bf16 v[184:187], v[228:231], v[116:119], v[184:187]\n"
      "v_exp_f32_e32 v163, v163\n"
      "v_mfma_f32_16x16x32_bf16 v[180:183], v[228:231], v[100:103], v[180:183]\n"
      "ds_read_b128 v[228:231], %1 offset:18176\n"
      "v_cvt_pk_bf16_f32 v198, v160, v161\n"
      "s_waitcnt lgkmcnt(2)\n"
      "v_mfma_f32_16x16x32_bf16 v[168:171], v[216:219], v[104:107], v[168:171]\n"
      "v_add_f32_e32 v233, v233, v160\n"
      "v_mfma_f32_16x16x32_bf16 v[172:175], v[216:219], v[120:123], v[172:175]\n"
      "v_exp_f32_e32 v164, v164\n"
      "v_add_f32_e32 v233, v233, v161\n"
      "v_cvt_pk_bf16_f32 v199, v162, v163\n"
      "v_mfma_f32_16x16x32_bf16 v[176:179], v[216:219], v[136:139], v[176:179]\n"
      "ds_read_b64_tr_b16 v[216:217], %2 offset:16384\n"
      "ds_read_b64_tr_b16 v[218:219], %2 offset:17408\n"
      "v_mfma_f32_16x16x32_bf16 v[188:191], v[220:223], v[136:139], v[188:191]\n"
      "v_exp_f32_e32 v165, v165\n"
      "v_mfma_f32_16x16x32_bf16 v[184:187], v[220:223], v[120:123], v[184:187]\n"
      "v_add_f32_e32 v233, v233, v162\n"
      "v_add_f32_e32 v233, v233, v163\n"
      "v_mfma_f32_16x16x32_bf16 v[180:183], v[220:223], v[104:107], v[180:183]\n"
      "ds_read_b64_tr_b16 v[220:221], %3 offset:16384\n"
      "ds_read_b64_tr_b16 v[222:223], %3 offset:17408\n"
      "s_waitcnt lgkmcnt(4)\n"
      "v_mfma_f32_16x16x32_bf16 v[168:171], v[224:227], v[108:111], v[168:171]\n"
      "v_exp_f32_e32 v166, v166\n"
      "v_cvt_pk_bf16_f32 v202, v164, v165\n"
      "v_mfma_f32_16x16x32_bf16 v[172:175], v[224:227], v[124:127], v[172:175]\n"
      "v_add_f32_e32 v234, v234, v164\n"
      "v_exp_f32_e32 v167, v167\n"
      "v_mfma_f32_16x16x32_bf16 v[176:179], v[224:227], v[140:143], v[176:179]\n"
      "ds_read_b64_tr_b16 v[224:225], %2 offset:16640\n"
      "ds_read_b64_tr_b16 v[226:227], %2 offset:17664\n"
      "v_mfma_f32_16x16x32_bf16 v[188:191], v[228:231], v[140:143], v[188:191]\n"
      "v_add_f32_e32 v234, v234, v165\n"
      "v_cvt_pk_bf16_f32 v203, v166, v167\n"
      "v_mfma_f32_16x16x32_bf16 v[184:187], v[228:231], v[124:127], v[184:187]\n"
      "v_add_f32_e32 v234, v234, v166\n"
      "v_add_f32_e32 v234, v234, v167\n"
      "v_mfma_f32_16x16x32_bf16 v[180:183], v[228:231], v[108:111], v[180:183]\n"
      "ds_read_b64_tr_b16 v[228:229], %3 offset:16640\n"
      "ds_read_b64_tr_b16 v[230:231], %3 offset:17664\n"
      "s_waitcnt lgkmcnt(4)\n"
      "v_mfma_f32_16x16x32_bf16 v[0:3], v[216:219], v[192:195], v[0:3]\n"
      "v_exp_f32_e32 v168, v168\n"
      "v_mfma_f32_16x16x32_bf16 v[32:35], v[216:219], v[196:199], v[32:35]\n"
      "v_exp_f32_e32 v169, v169\n"
      "v_add_f32_e32 v232, v232, v168\n"
      "v_mfma_f32_16x16x32_bf16 v[64:67], v[216:219], v[200:203], v[64:67]\n"
      "ds_read_b64_tr_b16 v[216:217], %2 offset:16896\n"
      "ds_read_b64_tr_b16 v[218:219], %2 offset:17920\n"
      "v_mfma_f32_16x16x32_bf16 v[68:71], v[220:223], v[200:203], v[68:71]\n"
      "v_add_f32_e32 v232, v232, v169\n"
      "v_exp_f32_e32 v170, v170\n"
      "v_mfma_f32_16x16x32_bf16 v[36:39], v[220:223], v[196:199], v[36:39]\n"
      "v_exp_f32_e32 v171, v171\n"
      "v_mfma_f32_16x16x32_bf16 v[4:7], v[220:223], v[192:195], v[4:7]\n"
      "ds_read_b64_tr_b16 v[220:221], %3 offset:16896\n"
      "ds_read_b64_tr_b16 v[222:223], %3 offset:17920\n"
      "s_waitcnt lgkmcnt(4)\n"
      "v_mfma_f32_16x16x32_bf16 v[8:11], v[224:227], v[192:195], v[8:11]\n"
      "v_add_f32_e32 v232, v232, v170\n"
      "v_add_f32_e32 v232, v232, v171\n"
      "v_mfma_f32_16x16x32_bf16 v[40:43], v[224:227], v[196:199], v[40:43]\n"
      "v_exp_f32_e32 v172, v172\n"
      "v_mfma_f32_16x16x32_bf16 v[72:75], v[224:227], v[200:203], v[72:75]\n"
      "ds_read_b64_tr_b16 v[224:225], %2 offset:17152\n"
      "ds_read_b64_tr_b16 v[226:227], %2 offset:18176\n"
      "v_mfma_f32_16x16x32_bf16 v[76:79], v[228:231], v[200:203], v[76:79]\n"
      "v_exp_f32_e32 v173, v173\n"
      "v_add_f32_e32 v233, v233, v172\n"
      "v_add_f32_e32 v233, v233, v173\n"
      "v_mfma_f32_16x16x32_bf16 v[44:47], v[228:231], v[196:199], v[44:47]\n"
      "v_exp_f32_e32 v174, v174\n"
      "v_mfma_f32_16x16x32_bf16 v[12:15], v[228:231], v[192:195], v[12:15]\n"
      "ds_read_b64_tr_b16 v[228:229], %3 offset:17152\n"
      "ds_read_b64_tr_b16 v[230:231], %3 offset:18176\n"
      "s_waitcnt lgkmcnt(4)\n"
      "v_mfma_f32_16x16x32_bf16 v[16:19], v[216:219], v[192:195], v[16:19]\n"
      "v_exp_f32_e32 v175, v175\n"
      "v_mfma_f32_16x16x32_bf16 v[48:51], v[216:219], v[196:199], v[48:51]\n"
      "v_add_f32_e32 v233, v233, v174\n"
      "v_add_f32_e32 v233, v233, v175\n"
      "v_mfma_f32_16x16x32_bf16 v[80:83], v[216:219], v[200:203], v[80:83]\n"
      "v_exp_f32_e32 v176, v176\n"
      "v_mfma_f32_16x16x32_bf16 v[84:87], v[220:223], v[200:203], v[84:87]\n"
      "s_waitcnt lgkmcnt(0)\n"
      "s_barrier\n"
      "ds_read_b128 v[216:219], %1 offset:32768\n"
      "v_mfma_f32_16x16x32_bf16 v[52:55], v[220:223], v[196:199], v[52:55]\n"
      "v_exp_f32_e32 v177, v177\n"
      "v_add_f32_e32 v234, v234, v176\n"
      "v_add_f32_e32 v234, v234, v177\n"
      "v_mfma_f32_16x16x32_bf16 v[20:23], v[220:223], v[192:195], v[20:23]\n"
      "ds_read_b128 v[220:223], %1 offset:33792\n"
      "v_mfma_f32_16x16x32_bf16 v[24:27], v[224:227], v[192:195], v[24:27]\n"
      "v_exp_f32_e32 v178, v178\n"
      "v_mfma_f32_16x16x32_bf16 v[56:59], v[224:227], v[196:199], v[56:59]\n"
      "v_exp_f32_e32 v179, v179\n"
      "v_add_f32_e32 v234, v234, v178\n"
      "v_add_f32_e32 v234, v234, v179\n"
      "v_mfma_f32_16x16x32_bf16 v[88:91], v[224:227], v[200:203], v[88:91]\n"
      "ds_read_b128 v[224:227], %1 offset:33024\n"
      "v_mfma_f32_16x16x32_bf16 v[92:95], v[228:231], v[200:203], v[92:95]\n"
      "v_exp_f32_e32 v180, v180\n"
      "v_exp_f32_e32 v181, v181\n"
      "v_mfma_f32_16x16x32_bf16 v[60:63], v[228:231], v[196:199], v[60:63]\n"
      "v_add_f32_e32 v232, v232, v180\n"
      "v_add_f32_e32 v232, v232, v181\n"
      "v_mfma_f32_16x16x32_bf16 v[28:31], v[228:231], v[192:195], v[28:31]\n"
      "ds_read_b128 v[228:231], %1 offset:34048\n"
      "s_waitcnt lgkmcnt(2)\n"
      "v_mfma_f32_16x16x32_bf16 v[144:147], v[216:219], v[96:99], v[204:207]\n"
      "v_exp_f32_e32 v182, v182\n"
      "v_mfma_f32_16x16x32_bf16 v[148:151], v[216:219], v[112:115], v[208:211]\n"
      "v_cvt_pk_bf16_f32 v192, v168, v169\n"
      "v_cvt_pk_bf16_f32 v193, v170, v171\n"
      "v_mfma_f32_16x16x32_bf16 v[152:155], v[216:219], v[128:131], v[212:215]\n"
      "ds_read_b128 v[216:219], %1 offset:33280\n"
      "v_exp_f32_e32 v183, v183\n"
      "v_mfma_f32_16x16x32_bf16 v[164:167], v[220:223], v[128:131], v[212:215]\n"
      "v_cvt_pk_bf16_f32 v196, v172, v173\n"
      "v_cvt_pk_bf16_f32 v197, v174, v175\n"
      "v_mfma_f32_16x16x32_bf16 v[160:163], v[220:223], v[112:115], v[208:211]\n"
      "v_exp_f32_e32 v184, v184\n"
      "v_cvt_pk_bf16_f32 v200, v176, v177\n"
      "v_mfma_f32_16x16x32_bf16 v[156:159], v[220:223], v[96:99], v[204:207]\n"
      "ds_read_b128 v[220:223], %1 offset:34304\n"
      "s_waitcnt lgkmcnt(2)\n"
      "v_mfma_f32_16x16x32_bf16 v[144:147], v[224:227], v[100:103], v[144:147]\n"
      "v_cvt_pk_bf16_f32 v201, v178, v179\n"
      "v_exp_f32_e32 v185, v185\n"
      "v_mfma_f32_16x16x32_bf16 v[148:151], v[224:227], v[116:119], v[148:151]\n"
      "v_cvt_pk_bf16_f32 v194, v180, v181\n"
      "v_cvt_pk_bf16_f32 v195, v182, v183\n"
      "v_mfma_f32_16x16x32_bf16 v[152:155], v[224:227], v[132:135], v[152:155]\n"
      "ds_read_b128 v[224:227], %1 offset:33536\n"
      "v_exp_f32_e32 v186, v186\n"
      "v_mfma_f32_16x16x32_bf16 v[164:167], v[228:231], v[132:135], v[164:167]\n"
      "v_add_f32_e32 v232, v232, v182\n"
      "v_add_f32_e32 v232, v232, v183\n"
      "v_mfma_f32_16x16x32_bf16 v[160:163], v[228:231], v[116:119], v[160:163]\n"
      "v_exp_f32_e32 v187, v187\n"
      "v_mfma_f32_16x16x32_bf16 v[156:159], v[228:231], v[100:103], v[156:159]\n"
      "ds_read_b128 v[228:231], %1 offset:34560\n"
      "v_cvt_pk_bf16_f32 v198, v184, v185\n"
      "s_waitcnt lgkmcnt(2)\n"
      "v_mfma_f32_16x16x32_bf16 v[144:147], v[216:219], v[104:107], v[144:147]\n"
      "v_add_f32_e32 v233, v233, v184\n"
      "v_mfma_f32_16x16x32_bf16 v[148:151], v[216:219], v[120:123], v[148:151]\n"
      "v_exp_f32_e32 v188, v188\n"
      "v_add_f32_e32 v233, v233, v185\n"
      "v_cvt_pk_bf16_f32 v199, v186, v187\n"
      "v_mfma_f32_16x16x32_bf16 v[152:155], v[216:219], v[136:139], v[152:155]\n"
      "ds_read_b64_tr_b16 v[216:217], %2 offset:32768\n"
      "ds_read_b64_tr_b16 v[218:219], %2 offset:33792\n"
      "v_mfma_f32_16x16x32_bf16 v[164:167], v[220:223], v[136:139], v[164:167]\n"
      "v_exp_f32_e32 v189, v189\n"
      "v_mfma_f32_16x16x32_bf16 v[160:163], v[220:223], v[120:123], v[160:163]\n"
      "v_add_f32_e32 v233, v233, v186\n"
      "v_add_f32_e32 v233, v233, v187\n"
      "v_mfma_f32_16x16x32_bf16 v[156:159], v[220:223], v[104:107], v[156:159]\n"
      "ds_read_b64_tr_b16 v[220:221], %3 offset:32768\n"
      "ds_read_b64_tr_b16 v[222:223], %3 offset:33792\n"
      "s_waitcnt lgkmcnt(4)\n"
      "v_mfma_f32_16x16x32_bf16 v[144:147], v[224:227], v[108:111], v[144:147]\n"
      "v_exp_f32_e32 v190, v190\n"
      "v_cvt_pk_bf16_f32 v202, v188, v189\n"
      "v_mfma_f32_16x16x32_bf16 v[148:151], v[224:227], v[124:127], v[148:151]\n"
      "v_add_f32_e32 v234, v234, v188\n"
      "v_exp_f32_e32 v191, v191\n"
      "v_mfma_f32_16x16x32_bf16 v[152:155], v[224:227], v[140:143], v[152:155]\n"
      "ds_read_b64_tr_b16 v[224:225], %2 offset:33024\n"
      "ds_read_b64_tr_b16 v[226:227], %2 offset:34048\n"
      "v_mfma_f32_16x16x32_bf16 v[164:167], v[228:231], v[140:143], v[164:167]\n"
      "v_add_f32_e32 v234, v234, v189\n"
      "v_cvt_pk_bf16_f32 v203, v190, v191\n"
      "v_mfma_f32_16x16x32_bf16 v[160:163], v[228:231], v[124:127], v[160:163]\n"
      "v_add_f32_e32 v234, v234, v190\n"
      "v_add_f32_e32 v234, v234, v191\n"
      "v_mfma_f32_16x16x32_bf16 v[156:159], v[228:231], v[108:111], v[156:159]\n"
      "ds_read_b64_tr_b16 v[228:229], %3 offset:33024\n"
      "ds_read_b64_tr_b16 v[230:231], %3 offset:34048\n"
      "s_waitcnt lgkmcnt(4)\n"
      "v_mfma_f32_16x16x32_bf16 v[0:3], v[216:219], v[192:195], v[0:3]\n"
      "v_exp_f32_e32 v144, v144\n"
      "v_mfma_f32_16x16x32_bf16 v[32:35], v[216:219], v[196:199], v[32:35]\n"
      "v_exp_f32_e32 v145, v145\n"
      "v_add_f32_e32 v232, v232, v144\n"
      "v_mfma_f32_16x16x32_bf16 v[64:67], v[216:219], v[200:203], v[64:67]\n"
      "ds_read_b64_tr_b16 v[216:217], %2 offset:33280\n"
      "ds_read_b64_tr_b16 v[218:219], %2 offset:34304\n"
      "v_mfma_f32_16x16x32_bf16 v[68:71], v[220:223], v[200:203], v[68:71]\n"
      "v_add_f32_e32 v232, v232, v145\n"
      "v_exp_f32_e32 v146, v146\n"
      "v_mfma_f32_16x16x32_bf16 v[36:39], v[220:223], v[196:199], v[36:39]\n"
      "v_exp_f32_e32 v147, v147\n"
      "v_mfma_f32_16x16x32_bf16 v[4:7], v[220:223], v[192:195], v[4:7]\n"
      "ds_read_b64_tr_b16 v[220:221], %3 offset:33280\n"
      "ds_read_b64_tr_b16 v[222:223], %3 offset:34304\n"
      "s_waitcnt lgkmcnt(4)\n"
      "v_mfma_f32_16x16x32_bf16 v[8:11], v[224:227], v[192:195], v[8:11]\n"
      "v_add_f32_e32 v232, v232, v146\n"
      "v_add_f32_e32 v232, v232, v147\n"
      "v_mfma_f32_16x16x32_bf16 v[40:43], v[224:227], v[196:199], v[40:43]\n"
      "v_exp_f32_e32 v148, v148\n"
      "v_mfma_f32_16x16x32_bf16 v[72:75], v[224:227], v[200:203], v[72:75]\n"
      "ds_read_b64_tr_b16 v[224:225], %2 offset:33536\n"
      "ds_read_b64_tr_b16 v[226:227], %2 offset:34560\n"
      "v_mfma_f32_16x16x32_bf16 v[76:79], v[228:231], v[200:203], v[76:79]\n"
      "v_exp_f32_e32 v149, v149\n"
      "v_add_f32_e32 v233, v233, v148\n"
      "v_add_f32_e32 v233, v233, v149\n"
      "v_mfma_f32_16x16x32_bf16 v[44:47], v[228:231], v[196:199], v[44:47]\n"
      "v_exp_f32_e32 v150, v150\n"
      "v_mfma_f32_16x16x32_bf16 v[12:15], v[228:231], v[192:195], v[12:15]\n"
      "ds_read_b64_tr_b16 v[228:229], %3 offset:33536\n"
      "ds_read_b64_tr_b16 v[230:231], %3 offset:34560\n"
      "s_waitcnt lgkmcnt(4)\n"
      "v_mfma_f32_16x16x32_bf16 v[16:19], v[216:219], v[192:195], v[16:19]\n"
      "v_exp_f32_e32 v151, v151\n"
      "v_mfma_f32_16x16x32_bf16 v[48:51], v[216:219], v[196:199], v[48:51]\n"
      "v_add_f32_e32 v233, v233, v150\n"
      "v_add_f32_e32 v233, v233, v151\n"
      "v_mfma_f32_16x16x32_bf16 v[80:83], v[216:219], v[200:203], v[80:83]\n"
      "v_exp_f32_e32 v152, v152\n"
      "v_mfma_f32_16x16x32_bf16 v[84:87], v[220:223], v[200:203], v[84:87]\n"
      "s_waitcnt lgkmcnt(0)\n"
      "s_barrier\n"
      "ds_read_b128 v[216:219], %1 offset:49152\n"
      "v_mfma_f32_16x16x32_bf16 v[52:55], v[220:223], v[196:199], v[52:55]\n"
      "v_exp_f32_e32 v153, v153\n"
      "v_add_f32_e32 v234, v234, v152\n"
      "v_add_f32_e32 v234, v234, v153\n"
      "v_mfma_f32_16x16x32_bf16 v[20:23], v[220:223], v[192:195], v[20:23]\n"
      "ds_read_b128 v[220:223], %1 offset:50176\n"
      "v_mfma_f32_16x16x32_bf16 v[24:27], v[224:227], v[192:195], v[24:27]\n"
      "v_exp_f32_e32 v154, v154\n"
      "v_mfma_f32_16x16x32_bf16 v[56:59], v[224:227], v[196:199], v[56:59]\n"
      "v_exp_f32_e32 v155, v155\n"
      "v_add_f32_e32 v234, v234, v154\n"
      "v_add_f32_e32 v234, v234, v155\n"
      "v_mfma_f32_16x16x32_bf16 v[88:91], v[224:227], v[200:203], v[88:91]\n"
      "ds_read_b128 v[224:227], %1 offset:49408\n"
      "v_mfma_f32_16x16x32_bf16 v[92:95], v[228:231], v[200:203], v[92:95]\n"
      "v_exp_f32_e32 v156, v156\n"
      "v_exp_f32_e32 v157, v157\n"
      "v_mfma_f32_16x16x32_bf16 v[60:63], v[228:231], v[196:199], v[60:63]\n"
      "v_add_f32_e32 v232, v232, v156\n"
      "v_add_f32_e32 v232, v232, v157\n"
      "v_mfma_f32_16x16x32_bf16 v[28:31], v[228:231], v[192:195], v[28:31]\n"
      "ds_read_b128 v[228:231], %1 offset:50432\n"
      "s_waitcnt lgkmcnt(2)\n"
      "v_mfma_f32_16x16x32_bf16 v[168:171], v[216:219], v[96:99], v[204:207]\n"
      "v_exp_f32_e32 v158, v158\n"
      "v_mfma_f32_16x16x32_bf16 v[172:175], v[216:219], v[112:115], v[208:211]\n"
      "v_cvt_pk_bf16_f32 v192, v144, v145\n"
      "v_cvt_pk_bf16_f32 v193, v146, v147\n"
      "v_mfma_f32_16x16x32_bf16 v[176:179], v[216:219], v[128:131], v[212:215]\n"
      "ds_read_b128 v[216:219], %1 offset:49664\n"
      "v_exp_f32_e32 v159, v159\n"
      "v_mfma_f32_16x16x32_bf16 v[188:191], v[220:223], v[128:131], v[212:215]\n"
      "v_cvt_pk_bf16_f32 v196, v148, v149\n"
      "v_cvt_pk_bf16_f32 v197, v150, v151\n"
      "v_mfma_f32_16x16x32_bf16 v[184:187], v[220:223], v[112:115], v[208:211]\n"
      "v_exp_f32_e32 v160, v160\n"
      "v_cvt_pk_bf16_f32 v200, v152, v153\n"
      "v_mfma_f32_16x16x32_bf16 v[180:183], v[220:223], v[96:99], v[204:207]\n"
      "ds_read_b128 v[220:223], %1 offset:50688\n"
      "s_waitcnt lgkmcnt(2)\n"
      "v_mfma_f32_16x16x32_bf16 v[168:171], v[224:227], v[100:103], v[168:171]\n"
      "v_cvt_pk_bf16_f32 v201, v154, v155\n"
      "v_exp_f32_e32 v161, v161\n"
      "v_mfma_f32_16x16x32_bf16 v[172:175], v[224:227], v[116:119], v[172:175]\n"
      "v_cvt_pk_bf16_f32 v194, v156, v157\n"
      "v_cvt_pk_bf16_f32 v195, v158, v159\n"
      "v_mfma_f32_16x16x32_bf16 v[176:179], v[224:227], v[132:135], v[176:179]\n"
      "ds_read_b128 v[224:227], %1 offset:49920\n"
      "v_exp_f32_e32 v162, v162\n"
      "v_mfma_f32_16x16x32_bf16 v[188:191], v[228:231], v[132:135], v[188:191]\n"
      "v_add_f32_e32 v232, v232, v158\n"
      "v_add_f32_e32 v232, v232, v159\n"
      "v_mfma_f32_16x16x32_bf16 v[184:187], v[228:231], v[116:119], v[184:187]\n"
      "v_exp_f32_e32 v163, v163\n"
      "v_mfma_f32_16x16x32_bf16 v[180:183], v[228:231], v[100:103], v[180:183]\n"
      "ds_read_b128 v[228:231], %1 offset:50944\n"
      "v_cvt_pk_bf16_f32 v198, v160, v161\n"
      "s_waitcnt lgkmcnt(2)\n"
      "v_mfma_f32_16x16x32_bf16 v[168:171], v[216:219], v[104:107], v[168:171]\n"
      "v_add_f32_e32 v233, v233, v160\n"
      "v_mfma_f32_16x16x32_bf16 v[172:175], v[216:219], v[120:123], v[172:175]\n"
      "v_exp_f32_e32 v164, v164\n"
      "v_add_f32_e32 v233, v233, v161\n"
      "v_cvt_pk_bf16_f32 v199, v162, v163\n"
      "v_mfma_f32_16x16x32_bf16 v[176:179], v[216:219], v[136:139], v[176:179]\n"
      "ds_read_b64_tr_b16 v[216:217], %2 offset:49152\n"
      "ds_read_b64_tr_b16 v[218:219], %2 offset:50176\n"
      "v_mfma_f32_16x16x32_bf16 v[188:191], v[220:223], v[136:139], v[188:191]\n"
      "v_exp_f32_e32 v165, v165\n"
      "v_mfma_f32_16x16x32_bf16 v[184:187], v[220:223], v[120:123], v[184:187]\n"
      "v_add_f32_e32 v233, v233, v162\n"
      "v_add_f32_e32 v233, v233, v163\n"
      "v_mfma_f32_16x16x32_bf16 v[180:183], v[220:223], v[104:107], v[180:183]\n"
      "ds_read_b64_tr_b16 v[220:221], %3 offset:49152\n"
      "ds_read_b64_tr_b16 v[222:223], %3 offset:50176\n"
      "s_waitcnt lgkmcnt(4)\n"
      "v_mfma_f32_16x16x32_bf16 v[168:171], v[224:227], v[108:111], v[168:171]\n"
      "v_exp_f32_e32 v166, v166\n"
      "v_cvt_pk_bf16_f32 v202, v164, v165\n"
      "v_mfma_f32_16x16x32_bf16 v[172:175], v[224:227], v[124:127], v[172:175]\n"
      "v_add_f32_e32 v234, v234, v164\n"
      "v_exp_f32_e32 v167, v167\n"
      "v_mfma_f32_16x16x32_bf16 v[176:179], v[224:227], v[140:143], v[176:179]\n"
      "ds_read_b64_tr_b16 v[224:225], %2 offset:49408\n"
      "ds_read_b64_tr_b16 v[226:227], %2 offset:50432\n"
      "v_mfma_f32_16x16x32_bf16 v[188:191], v[228:231], v[140:143], v[188:191]\n"
      "v_add_f32_e32 v234, v234, v165\n"
      "v_cvt_pk_bf16_f32 v203, v166, v167\n"
      "v_mfma_f32_16x16x32_bf16 v[184:187], v[228:231], v[124:127], v[184:187]\n"
      "v_add_f32_e32 v234, v234, v166\n"
      "v_add_f32_e32 v234, v234, v167\n"
      "v_mfma_f32_16x16x32_bf16 v[180:183], v[228:231], v[108:111], v[180:183]\n"
      "ds_read_b64_tr_b16 v[228:229], %3 offset:49408\n"
      "ds_read_b64_tr_b16 v[230:231], %3 offset:50432\n"
      "s_waitcnt lgkmcnt(4)\n"
      "v_mfma_f32_16x16x32_bf16 v[0:3], v[216:219], v[192:195], v[0:3]\n"
      "v_exp_f32_e32 v168, v168\n"
      "v_mfma_f32_16x16x32_bf16 v[32:35], v[216:219], v[196:199], v[32:35]\n"
      "v_exp_f32_e32 v169, v169\n"
      "v_add_f32_e32 v232, v232, v168\n"
      "v_mfma_f32_16x16x32_bf16 v[64:67], v[216:219], v[200:203], v[64:67]\n"
      "ds_read_b64_tr_b16 v[216:217], %2 offset:49664\n"
      "ds_read_b64_tr_b16 v[218:219], %2 offset:50688\n"
      "v_mfma_f32_16x16x32_bf16 v[68:71], v[220:223], v[200:203], v[68:71]\n"
      "v_add_f32_e32 v232, v232, v169\n"
      "v_exp_f32_e32 v170, v170\n"
      "v_mfma_f32_16x16x32_bf16 v[36:39], v[220:223], v[196:199], v[36:39]\n"
      "v_exp_f32_e32 v171, v171\n"
      "v_mfma_f32_16x16x32_bf16 v[4:7], v[220:223], v[192:195], v[4:7]\n"
      "ds_read_b64_tr_b16 v[220:221], %3 offset:49664\n"
      "ds_read_b64_tr_b16 v[222:223], %3 offset:50688\n"
      "s_waitcnt lgkmcnt(4)\n"
      "v_mfma_f32_16x16x32_bf16 v[8:11], v[224:227], v[192:195], v[8:11]\n"
      "v_add_f32_e32 v232, v232, v170\n"
      "v_add_f32_e32 v232, v232, v171\n"
      "v_mfma_f32_16x16x32_bf16 v[40:43], v[224:227], v[196:199], v[40:43]\n"
      "v_exp_f32_e32 v172, v172\n"
      "v_mfma_f32_16x16x32_bf16 v[72:75], v[224:227], v[200:203], v[72:75]\n"
      "ds_read_b64_tr_b16 v[224:225], %2 offset:49920\n"
      "ds_read_b64_tr_b16 v[226:227], %2 offset:50944\n"
      "v_mfma_f32_16x16x32_bf16 v[76:79], v[228:231], v[200:203], v[76:79]\n"
      "v_exp_f32_e32 v173, v173\n"
      "v_add_f32_e32 v233, v233, v172\n"
      "v_add_f32_e32 v233, v233, v173\n"
      "v_mfma_f32_16x16x32_bf16 v[44:47], v[228:231], v[196:199], v[44:47]\n"
      "v_exp_f32_e32 v174, v174\n"
      "v_mfma_f32_16x16x32_bf16 v[12:15], v[228:231], v[192:195], v[12:15]\n"
      "ds_read_b64_tr_b16 v[228:229], %3 offset:49920\n"
      "ds_read_b64_tr_b16 v[230:231], %3 offset:50944\n"
      "s_waitcnt lgkmcnt(4)\n"
      "v_mfma_f32_16x16x32_bf16 v[16:19], v[216:219], v[192:195], v[16:19]\n"
      "v_exp_f32_e32 v175, v175\n"
      "v_mfma_f32_16x16x32_bf16 v[48:51], v[216:219], v[196:199], v[48:51]\n"
      "v_add_f32_e32 v233, v233, v174\n"
      "v_add_f32_e32 v233, v233, v175\n"
      "v_mfma_f32_16x16x32_bf16 v[80:83], v[216:219], v[200:203], v[80:83]\n"
      "v_exp_f32_e32 v176, v176\n"
      "v_mfma_f32_16x16x32_bf16 v[84:87], v[220:223], v[200:203], v[84:87]\n"
      "s_waitcnt lgkmcnt(0)\n"
      "s_barrier\n"
      "ds_read_b128 v[216:219], %1 offset:0\n"
      "v_mfma_f32_16x16x32_bf16 v[52:55], v[220:223], v[196:199], v[52:55]\n"
      "v_exp_f32_e32 v177, v177\n"
      "v_add_f32_e32 v234, v234, v176\n"
      "v_add_f32_e32 v234, v234, v177\n"
      "v_mfma_f32_16x16x32_bf16 v[20:23], v[220:223], v[192:195], v[20:23]\n"
      "ds_read_b128 v[220:223], %1 offset:1024\n"
      "v_mfma_f32_16x16x32_bf16 v[24:27], v[224:227], v[192:195], v[24:27]\n"
      "v_exp_f32_e32 v178, v178\n"
      "v_mfma_f32_16x16x32_bf16 v[56:59], v[224:227], v[196:199], v[56:59]\n"
      "v_exp_f32_e32 v179, v179\n"
      "v_add_f32_e32 v234, v234, v178\n"
      "v_add_f32_e32 v234, v234, v179\n"
      "v_mfma_f32_16x16x32_bf16 v[88:91], v[224:227], v[200:203], v[88:91]\n"
      "ds_read_b128 v[224:227], %1 offset:256\n"
      "v_mfma_f32_16x16x32_bf16 v[92:95], v[228:231], v[200:203], v[92:95]\n"
      "v_exp_f32_e32 v180, v180\n"
      "v_exp_f32_e32 v181, v181\n"
      "v_mfma_f32_16x16x32_bf16 v[60:63], v[228:231], v[196:199], v[60:63]\n"
      "v_add_f32_e32 v232, v232, v180\n"
      "v_add_f32_e32 v232, v232, v181\n"
      "v_mfma_f32_16x16x32_bf16 v[28:31], v[228:231], v[192:195], v[28:31]\n"
      "ds_read_b128 v[228:231], %1 offset:1280\n"
      "s_waitcnt lgkmcnt(2)\n"
      "v_mfma_f32_16x16x32_bf16 v[144:147], v[216:219], v[96:99], v[204:207]\n"
      "v_exp_f32_e32 v182, v182\n"
      "v_mfma_f32_16x16x32_bf16 v[148:151], v[216:219], v[112:115], v[208:211]\n"
      "v_cvt_pk_bf16_f32 v192, v168, v169\n"
      "v_cvt_pk_bf16_f32 v193, v170, v171\n"
      "v_mfma_f32_16x16x32_bf16 v[152:155], v[216:219], v[128:131], v[212:215]\n"
      "ds_read_b128 v[216:219], %1 offset:512\n"
      "v_exp_f32_e32 v183, v183\n"
      "v_mfma_f32_16x16x32_bf16 v[164:167], v[220:223], v[128:131], v[212:215]\n"
      "v_cvt_pk_bf16_f32 v196, v172, v173\n"
      "v_cvt_pk_bf16_f32 v197, v174, v175\n"
      "v_mfma_f32_16x16x32_bf16 v[160:163], v[220:223], v[112:115], v[208:211]\n"
      "v_exp_f32_e32 v184, v184\n"
      "v_cvt_pk_bf16_f32 v200, v176, v177\n"
      "v_mfma_f32_16x16x32_bf16 v[156:159], v[220:223], v[96:99], v[204:207]\n"
      "ds_read_b128 v[220:223], %1 offset:1536\n"
      "s_waitcnt lgkmcnt(2)\n"
      "v_mfma_f32_16x16x32_bf16 v[144:147], v[224:227], v[100:103], v[144:147]\n"
      "v_cvt_pk_bf16_f32 v201, v178, v179\n"
      "v_exp_f32_e32 v185, v185\n"
      "v_mfma_f32_16x16x32_bf16 v[148:151], v[224:227], v[116:119], v[148:151]\n"
      "v_cvt_pk_bf16_f32 v194, v180, v181\n"
      "v_cvt_pk_bf16_f32 v195, v182, v183\n"
      "v_mfma_f32_16x16x32_bf16 v[152:155], v[224:227], v[132:135], v[152:155]\n"
      "ds_read_b128 v[224:227], %1 offset:768\n"
      "v_exp_f32_e32 v186, v186\n"
      "v_mfma_f32_16x16x32_bf16 v[164:167], v[228:231], v[132:135], v[164:167]\n"
      "v_add_f32_e32 v232, v232, v182\n"
      "v_add_f32_e32 v232, v232, v183\n"
      "v_mfma_f32_16x16x32_bf16 v[160:163], v[228:231], v[116:119], v[160:163]\n"
      "v_exp_f32_e32 v187, v187\n"
      "v_mfma_f32_16x16x32_bf16 v[156:159], v[228:231], v[100:103], v[156:159]\n"
      "ds_read_b128 v[228:231], %1 offset:1792\n"
      "v_cvt_pk_bf16_f32 v198, v184, v185\n"
      "s_waitcnt lgkmcnt(2)\n"
      "v_mfma_f32_16x16x32_bf16 v[144:147], v[216:219], v[104:107], v[144:147]\n"
      "v_add_f32_e32 v233, v233, v184\n"
      "v_mfma_f32_16x16x32_bf16 v[148:151], v[216:219], v[120:123], v[148:151]\n"
      "v_exp_f32_e32 v188, v188\n"
      "v_add_f32_e32 v233, v233, v185\n"
      "v_cvt_pk_bf16_f32 v199, v186, v187\n"
      "v_mfma_f32_16x16x32_bf16 v[152:155], v[216:219], v[136:139], v[152:155]\n"
      "ds_read_b64_tr_b16 v[216:217], %2 offset:0\n"
      "ds_read_b64_tr_b16 v[218:219], %2 offset:1024\n"
      "v_mfma_f32_16x16x32_bf16 v[164:167], v[220:223], v[136:139], v[164:167]\n"
      "v_exp_f32_e32 v189, v189\n"
      "v_mfma_f32_16x16x32_bf16 v[160:163], v[220:223], v[120:123], v[160:163]\n"
      "v_add_f32_e32 v233, v233, v186\n"
      "v_add_f32_e32 v233, v233, v187\n"
      "v_mfma_f32_16x16x32_bf16 v[156:159], v[220:223], v[104:107], v[156:159]\n"
      "ds_read_b64_tr_b16 v[220:221], %3 offset:0\n"
      "ds_read_b64_tr_b16 v[222:223], %3 offset:1024\n"
      "s_waitcnt lgkmcnt(4)\n"
      "v_mfma_f32_16x16x32_bf16 v[144:147], v[224:227], v[108:111], v[144:147]\n"
      "v_exp_f32_e32 v190, v190\n"
      "v_cvt_pk_bf16_f32 v202, v188, v189\n"
      "v_mfma_f32_16x16x32_bf16 v[148:151], v[224:227], v[124:127], v[148:151]\n"
      "v_add_f32_e32 v234, v234, v188\n"
      "v_exp_f32_e32 v191, v191\n"
      "v_mfma_f32_16x16x32_bf16 v[152:155], v[224:227], v[140:143], v[152:155]\n"
      "ds_read_b64_tr_b16 v[224:225], %2 offset:256\n"
      "ds_read_b64_tr_b16 v[226:227], %2 offset:1280\n"
      "v_mfma_f32_16x16x32_bf16 v[164:167], v[228:231], v[140:143], v[164:167]\n"
      "v_add_f32_e32 v234, v234, v189\n"
      "v_cvt_pk_bf16_f32 v203, v190, v191\n"
      "v_mfma_f32_16x16x32_bf16 v[160:163], v[228:231], v[124:127], v[160:163]\n"
      "v_add_f32_e32 v234, v234, v190\n"
      "v_add_f32_e32 v234, v234, v191\n"
      "v_mfma_f32_16x16x32_bf16 v[156:159], v[228:231], v[108:111], v[156:159]\n"
      "ds_read_b64_tr_b16 v[228:229], %3 offset:256\n"
      "ds_read_b64_tr_b16 v[230:231], %3 offset:1280\n"
      "s_waitcnt lgkmcnt(4)\n"
      "v_mfma_f32_16x16x32_bf16 v[0:3], v[216:219], v[192:195], v[0:3]\n"
      "v_exp_f32_e32 v144, v144\n"
      "v_mfma_f32_16x16x32_bf16 v[32:35], v[216:219], v[196:199], v[32:35]\n"
      "v_exp_f32_e32 v145, v145\n"
      "v_add_f32_e32 v232, v232, v144\n"
      "v_mfma_f32_16x16x32_bf16 v[64:67], v[216:219], v[200:203], v[64:67]\n"
      "ds_read_b64_tr_b16 v[216:217], %2 offset:512\n"
      "ds_read_b64_tr_b16 v[218:219], %2 offset:1536\n"
      "v_mfma_f32_16x16x32_bf16 v[68:71], v[220:223], v[200:203], v[68:71]\n"
      "v_add_f32_e32 v232, v232, v145\n"
      "v_exp_f32_e32 v146, v146\n"
      "v_mfma_f32_16x16x32_bf16 v[36:39], v[220:223], v[196:199], v[36:39]\n"
      "v_exp_f32_e32 v147, v147\n"
      "v_mfma_f32_16x16x32_bf16 v[4:7], v[220:223], v[192:195], v[4:7]\n"
      "ds_read_b64_tr_b16 v[220:221], %3 offset:512\n"
      "ds_read_b64_tr_b16 v[222:223], %3 offset:1536\n"
      "s_waitcnt lgkmcnt(4)\n"
      "v_mfma_f32_16x16x32_bf16 v[8:11], v[224:227], v[192:195], v[8:11]\n"
      "v_add_f32_e32 v232, v232, v146\n"
      "v_add_f32_e32 v232, v232, v147\n"
      "v_mfma_f32_16x16x32_bf16 v[40:43], v[224:227], v[196:199], v[40:43]\n"
      "v_exp_f32_e32 v148, v148\n"
      "v_mfma_f32_16x16x32_bf16 v[72:75], v[224:227], v[200:203], v[72:75]\n"
      "ds_read_b64_tr_b16 v[224:225], %2 offset:768\n"
      "ds_read_b64_tr_b16 v[226:227], %2 offset:1792\n"
      "v_mfma_f32_16x16x32_bf16 v[76:79], v[228:231], v[200:203], v[76:79]\n"
      "v_exp_f32_e32 v149, v149\n"
      "v_add_f32_e32 v233, v233, v148\n"
      "v_add_f32_e32 v233, v233, v149\n"
      "v_mfma_f32_16x16x32_bf16 v[44:47], v[228:231], v[196:199], v[44:47]\n"
      "v_exp_f32_e32 v150, v150\n"
      "v_mfma_f32_16x16x32_bf16 v[12:15], v[228:231], v[192:195], v[12:15]\n"
      "ds_read_b64_tr_b16 v[228:229], %3 offset:768\n"
      "ds_read_b64_tr_b16 v[230:231], %3 offset:1792\n"
      "s_waitcnt lgkmcnt(4)\n"
      "v_mfma_f32_16x16x32_bf16 v[16:19], v[216:219], v[192:195], v[16:19]\n"
      "v_exp_f32_e32 v151, v151\n"
      "v_mfma_f32_16x16x32_bf16 v[48:51], v[216:219], v[196:199], v[48:51]\n"
      "v_add_f32_e32 v233, v233, v150\n"
      "v_add_f32_e32 v233, v233, v151\n"
      "v_mfma_f32_16x16x32_bf16 v[80:83], v[216:219], v[200:203], v[80:83]\n"
      "v_exp_f32_e32 v152, v152\n"
      "v_mfma_f32_16x16x32_bf16 v[84:87], v[220:223], v[200:203], v[84:87]\n"
      "s_waitcnt lgkmcnt(0)\n"
      "s_barrier\n"
      "ds_read_b128 v[216:219], %1 offset:16384\n"
      "v_mfma_f32_16x16x32_bf16 v[52:55], v[220:223], v[196:199], v[52:55]\n"
      "v_exp_f32_e32 v153, v153\n"
      "v_add_f32_e32 v234, v234, v152\n"
      "v_add_f32_e32 v234, v234, v153\n"
      "v_mfma_f32_16x16x32_bf16 v[20:23], v[220:223], v[192:195], v[20:23]\n"
      "ds_read_b128 v[220:223], %1 offset:17408\n"
      "v_mfma_f32_16x16x32_bf16 v[24:27], v[224:227], v[192:195], v[24:27]\n"
      "v_exp_f32_e32 v154, v154\n"
      "v_mfma_f32_16x16x32_bf16 v[56:59], v[224:227], v[196:199], v[56:59]\n"
      "v_exp_f32_e32 v155, v155\n"
      "v_add_f32_e32 v234, v234, v154\n"
      "v_add_f32_e32 v234, v234, v155\n"
      "v_mfma_f32_16x16x32_bf16 v[88:91], v[224:227], v[200:203], v[88:91]\n"
      "ds_read_b128 v[224:227], %1 offset:16640\n"
      "v_mfma_f32_16x16x32_bf16 v[92:95], v[228:231], v[200:203], v[92:95]\n"
      "v_exp_f32_e32 v156, v156\n"
      "v_exp_f32_e32 v157, v157\n"
      "v_mfma_f32_16x16x32_bf16 v[60:63], v[228:231], v[196:199], v[60:63]\n"
      "v_add_f32_e32 v232, v232, v156\n"
      "v_add_f32_e32 v232, v232, v157\n"
      "v_mfma_f32_16x16x32_bf16 v[28:31], v[228:231], v[192:195], v[28:31]\n"
      "ds_read_b128 v[228:231], %1 offset:17664\n"
      "s_sub_u32 s41, s41, 1\n"
      "s_cmp_lg_u32 s41, 0\n"
      "s_cbranch_scc1 LLOOP%=\n"
      "s_waitcnt lgkmcnt(2)\n"
      "v_mfma_f32_16x16x32_bf16 v[168:171], v[216:219], v[96:99], v[204:207]\n"
      "v_exp_f32_e32 v158, v158\n"
      "v_mfma_f32_16x16x32_bf16 v[172:175], v[216:219], v[112:115], v[208:211]\n"
      "v_cvt_pk_bf16_f32 v192, v144, v145\n"
      "v_cvt_pk_bf16_f32 v193, v146, v147\n"
      "v_mfma_f32_16x16x32_bf16 v[176:179], v[216:219], v[128:131], v[212:215]\n"
      "ds_read_b128 v[216:219], %1 offset:16896\n"
      "v_exp_f32_e32 v159, v159\n"
      "v_mfma_f32_16x16x32_bf16 v[188:191], v[220:223], v[128:131], v[212:215]\n"
      "v_cvt_pk_bf16_f32 v196, v148, v149\n"
      "v_cvt_pk_bf16_f32 v197, v150, v151\n"
      "v_mfma_f32_16x16x32_bf16 v[184:187], v[220:223], v[112:115], v[208:211]\n"
      "v_exp_f32_e32 v160, v160\n"
      "v_cvt_pk_bf16_f32 v200, v152, v153\n"
      "v_mfma_f32_16x16x32_bf16 v[180:183], v[220:223], v[96:99], v[204:207]\n"
      "ds_read_b128 v[220:223], %1 offset:17920\n"
      "s_waitcnt lgkmcnt(2)\n"
      "v_mfma_f32_16x16x32_bf16 v[168:171], v[224:227], v[100:103], v[168:171]\n"
      "v_cvt_pk_bf16_f32 v201, v154, v155\n"
      "v_exp_f32_e32 v161, v161\n"
      "v_mfma_f32_16x16x32_bf16 v[172:175], v[224:227], v[116:119], v[172:175]\n"
      "v_cvt_pk_bf16_f32 v194, v156, v157\n"
      "v_cvt_pk_bf16_f32 v195, v158, v159\n"
      "v_mfma_f32_16x16x32_bf16 v[176:179], v[224:227], v[132:135], v[176:179]\n"
      "ds_read_b128 v[224:227], %1 offset:17152\n"
      "v_exp_f32_e32 v162, v162\n"
      "v_mfma_f32_16x16x32_bf16 v[188:191], v[228:231], v[132:135], v[188:191]\n"
      "v_add_f32_e32 v232, v232, v158\n"
      "v_add_f32_e32 v232, v232, v159\n"
      "v_mfma_f32_16x16x32_bf16 v[184:187], v[228:231], v[116:119], v[184:187]\n"
      "v_exp_f32_e32 v163, v163\n"
      "v_mfma_f32_16x16x32_bf16 v[180:183], v[228:231], v[100:103], v[180:183]\n"
      "ds_read_b128 v[228:231], %1 offset:18176\n"
      "v_cvt_pk_bf16_f32 v198, v160, v161\n"
      "s_waitcnt lgkmcnt(2)\n"
      "v_mfma_f32_16x16x32_bf16 v[168:171], v[216:219], v[104:107], v[168:171]\n"
      "v_add_f32_e32 v233, v233, v160\n"
      "v_mfma_f32_16x16x32_bf16 v[172:175], v[216:219], v[120:123], v[172:175]\n"
      "v_exp_f32_e32 v164, v164\n"
      "v_add_f32_e32 v233, v233, v161\n"
      "v_cvt_pk_bf16_f32 v199, v162, v163\n"
      "v_mfma_f32_16x16x32_bf16 v[176:179], v[216:219], v[136:139], v[176:179]\n"
      "ds_read_b64_tr_b16 v[216:217], %2 offset:16384\n"
      "ds_read_b64_tr_b16 v[218:219], %2 offset:17408\n"
      "v_mfma_f32_16x16x32_bf16 v[188:191], v[220:223], v[136:139], v[188:191]\n"
      "v_exp_f32_e32 v165, v165\n"
      "v_mfma_f32_16x16x32_bf16 v[184:187], v[220:223], v[120:123], v[184:187]\n"
      "v_add_f32_e32 v233, v233, v162\n"
      "v_add_f32_e32 v233, v233, v163\n"
      "v_mfma_f32_16x16x32_bf16 v[180:183], v[220:223], v[104:107], v[180:183]\n"
      "ds_read_b64_tr_b16 v[220:221], %3 offset:16384\n"
      "ds_read_b64_tr_b16 v[222:223], %3 offset:17408\n"
      "s_waitcnt lgkmcnt(4)\n"
      "v_mfma_f32_16x16x32_bf16 v[168:171], v[224:227], v[108:111], v[168:171]\n"
      "v_exp_f32_e32 v166, v166\n"
      "v_cvt_pk_bf16_f32 v202, v164, v165\n"
      "v_mfma_f32_16x16x32_bf16 v[172:175], v[224:227], v[124:127], v[172:175]\n"
      "v_add_f32_e32 v234, v234, v164\n"
      "v_exp_f32_e32 v167, v167\n"
      "v_mfma_f32_16x16x32_bf16 v[176:179], v[224:227], v[140:143], v[176:179]\n"
      "ds_read_b64_tr_b16 v[224:225], %2 offset:16640\n"
      "ds_read_b64_tr_b16 v[226:227], %2 offset:17664\n"
      "v_mfma_f32_16x16x32_bf16 v[188:191], v[228:231], v[140:143], v[188:191]\n"
      "v_add_f32_e32 v234, v234, v165\n"
      "v_cvt_pk_bf16_f32 v203, v166, v167\n"
      "v_mfma_f32_16x16x32_bf16 v[184:187], v[228:231], v[124:127], v[184:187]\n"
      "v_add_f32_e32 v234, v234, v166\n"
      "v_add_f32_e32 v234, v234, v167\n"
      "v_mfma_f32_16x16x32_bf16 v[180:183], v[228:231], v[108:111], v[180:183]\n"
      "ds_read_b64_tr_b16 v[228:229], %3 offset:16640\n"
      "ds_read_b64_tr_b16 v[230:231], %3 offset:17664\n"
      "s_waitcnt lgkmcnt(4)\n"
      "v_mfma_f32_16x16x32_bf16 v[0:3], v[216:219], v[192:195], v[0:3]\n"
      "v_exp_f32_e32 v168, v168\n"
      "v_mfma_f32_16x16x32_bf16 v[32:35], v[216:219], v[196:199], v[32:35]\n"
      "v_exp_f32_e32 v169, v169\n"
      "v_add_f32_e32 v232, v232, v168\n"
      "v_mfma_f32_16x16x32_bf16 v[64:67], v[216:219], v[200:203], v[64:67]\n"
      "ds_read_b64_tr_b16 v[216:217], %2 offset:16896\n"
      "ds_read_b64_tr_b16 v[218:219], %2 offset:17920\n"
      "v_mfma_f32_16x16x32_bf16 v[68:71], v[220:223], v[200:203], v[68:71]\n"
      "v_add_f32_e32 v232, v232, v169\n"
      "v_exp_f32_e32 v170, v170\n"
      "v_mfma_f32_16x16x32_bf16 v[36:39], v[220:223], v[196:199], v[36:39]\n"
      "v_exp_f32_e32 v171, v171\n"
      "v_mfma_f32_16x16x32_bf16 v[4:7], v[220:223], v[192:195], v[4:7]\n"
      "ds_read_b64_tr_b16 v[220:221], %3 offset:16896\n"
      "ds_read_b64_tr_b16 v[222:223], %3 offset:17920\n"
      "s_waitcnt lgkmcnt(4)\n"
      "v_mfma_f32_16x16x32_bf16 v[8:11], v[224:227], v[192:195], v[8:11]\n"
      "v_add_f32_e32 v232, v232, v170\n"
      "v_add_f32_e32 v232, v232, v171\n"
      "v_mfma_f32_16x16x32_bf16 v[40:43], v[224:227], v[196:199], v[40:43]\n"
      "v_exp_f32_e32 v172, v172\n"
      "v_mfma_f32_16x16x32_bf16 v[72:75], v[224:227], v[200:203], v[72:75]\n"
      "ds_read_b64_tr_b16 v[224:225], %2 offset:17152\n"
      "ds_read_b64_tr_b16 v[226:227], %2 offset:18176\n"
      "v_mfma_f32_16x16x32_bf16 v[76:79], v[228:231], v[200:203], v[76:79]\n"
      "v_exp_f32_e32 v173, v173\n"
      "v_add_f32_e32 v233, v233, v172\n"
      "v_add_f32_e32 v233, v233, v173\n"
      "v_mfma_f32_16x16x32_bf16 v[44:47], v[228:231], v[196:199], v[44:47]\n"
      "v_exp_f32_e32 v174, v174\n"
      "v_mfma_f32_16x16x32_bf16 v[12:15], v[228:231], v[192:195], v[12:15]\n"
      "ds_read_b64_tr_b16 v[228:229], %3 offset:17152\n"
      "ds_read_b64_tr_b16 v[230:231], %3 offset:18176\n"
      "s_waitcnt lgkmcnt(4)\n"
      "v_mfma_f32_16x16x32_bf16 v[16:19], v[216:219], v[192:195], v[16:19]\n"
      "v_exp_f32_e32 v175, v175\n"
      "v_mfma_f32_16x16x32_bf16 v[48:51], v[216:219], v[196:199], v[48:51]\n"
      "v_add_f32_e32 v233, v233, v174\n"
      "v_add_f32_e32 v233, v233, v175\n"
      "v_mfma_f32_16x16x32_bf16 v[80:83], v[216:219], v[200:203], v[80:83]\n"
      "v_exp_f32_e32 v176, v176\n"
      "v_mfma_f32_16x16x32_bf16 v[84:87], v[220:223], v[200:203], v[84:87]\n"
      "s_waitcnt lgkmcnt(0)\n"
      "s_barrier\n"
      "ds_read_b128 v[216:219], %1 offset:32768\n"
      "v_mfma_f32_16x16x32_bf16 v[52:55], v[220:223], v[196:199], v[52:55]\n"
      "v_exp_f32_e32 v177, v177\n"
      "v_add_f32_e32 v234, v234, v176\n"
      "v_add_f32_e32 v234, v234, v177\n"
      "v_mfma_f32_16x16x32_bf16 v[20:23], v[220:223], v[192:195], v[20:23]\n"
      "ds_read_b128 v[220:223], %1 offset:33792\n"
      "v_mfma_f32_16x16x32_bf16 v[24:27], v[224:227], v[192:195], v[24:27]\n"
      "v_exp_f32_e32 v178, v178\n"
      "v_mfma_f32_16x16x32_bf16 v[56:59], v[224:227], v[196:199], v[56:59]\n"
      "v_exp_f32_e32 v179, v179\n"
      "v_add_f32_e32 v234, v234, v178\n"
      "v_add_f32_e32 v234, v234, v179\n"
      "v_mfma_f32_16x16x32_bf16 v[88:91], v[224:227], v[200:203], v[88:91]\n"
      "ds_read_b128 v[224:227], %1 offset:33024\n"
      "v_mfma_f32_16x16x32_bf16 v[92:95], v[228:231], v[200:203], v[92:95]\n"
      "v_exp_f32_e32 v180, v180\n"
      "v_exp_f32_e32 v181, v181\n"
      "v_mfma_f32_16x16x32_bf16 v[60:63], v[228:231], v[196:199], v[60:63]\n"
      "v_add_f32_e32 v232, v232, v180\n"
      "v_add_f32_e32 v232, v232, v181\n"
      "v_mfma_f32_16x16x32_bf16 v[28:31], v[228:231], v[192:195], v[28:31]\n"
      "ds_read_b128 v[228:231], %1 offset:34048\n"
      "s_waitcnt lgkmcnt(2)\n"
      "v_mfma_f32_16x16x32_bf16 v[144:147], v[216:219], v[96:99], v[204:207]\n"
      "v_exp_f32_e32 v182, v182\n"
      "v_mfma_f32_16x16x32_bf16 v[148:151], v[216:219], v[112:115], v[208:211]\n"
      "v_cvt_pk_bf16_f32 v192, v168, v169\n"
      "v_cvt_pk_bf16_f32 v193, v170, v171\n"
      "v_mfma_f32_16x16x32_bf16 v[152:155], v[216:219], v[128:131], v[212:215]\n"
      "ds_read_b128 v[216:219], %1 offset:33280\n"
      "v_exp_f32_e32 v183, v183\n"
      "v_mfma_f32_16x16x32_bf16 v[164:167], v[220:223], v[128:131], v[212:215]\n"
      "v_cvt_pk_bf16_f32 v196, v172, v173\n"
      "v_cvt_pk_bf16_f32 v197, v174, v175\n"
      "v_mfma_f32_16x16x32_bf16 v[160:163], v[220:223], v[112:115], v[208:211]\n"
      "v_exp_f32_e32 v184, v184\n"
      "v_cvt_pk_bf16_f32 v200, v176, v177\n"
      "v_mfma_f32_16x16x32_bf16 v[156:159], v[220:223], v[96:99], v[204:207]\n"
      "ds_read_b128 v[220:223], %1 offset:34304\n"
      "s_waitcnt lgkmcnt(2)\n"
      "v_mfma_f32_16x16x32_bf16 v[144:147], v[224:227], v[100:103], v[144:147]\n"
      "v_cvt_pk_bf16_f32 v201, v178, v179\n"
      "v_exp_f32_e32 v185, v185\n"
      "v_mfma_f32_16x16x32_bf16 v[148:151], v[224:227], v[116:119], v[148:151]\n"
      "v_cvt_pk_bf16_f32 v194, v180, v181\n"
      "v_cvt_pk_bf16_f32 v195, v182, v183\n"
      "v_mfma_f32_16x16x32_bf16 v[152:155], v[224:227], v[132:135], v[152:155]\n"
      "ds_read_b128 v[224:227], %1 offset:33536\n"
      "v_exp_f32_e32 v186, v186\n"
      "v_mfma_f32_16x16x32_bf16 v[164:167], v[228:231], v[132:135], v[164:167]\n"
      "v_add_f32_e32 v232, v232, v182\n"
      "v_add_f32_e32 v232, v232, v183\n"
      "v_mfma_f32_16x16x32_bf16 v[160:163], v[228:231], v[116:119], v[160:163]\n"
      "v_exp_f32_e32 v187, v187\n"
      "v_mfma_f32_16x16x32_bf16 v[156:159], v[228:231], v[100:103], v[156:159]\n"
      "ds_read_b128 v[228:231], %1 offset:34560\n"
      "v_cvt_pk_bf16_f32 v198, v184, v185\n"
      "s_waitcnt lgkmcnt(2)\n"
      "v_mfma_f32_16x16x32_bf16 v[144:147], v[216:219], v[104:107], v[144:147]\n"
      "v_add_f32_e32 v233, v233, v184\n"
      "v_mfma_f32_16x16x32_bf16 v[148:151], v[216:219], v[120:123], v[148:151]\n"
      "v_exp_f32_e32 v188, v188\n"
      "v_add_f32_e32 v233, v233, v185\n"
      "v_cvt_pk_bf16_f32 v199, v186, v187\n"
      "v_mfma_f32_16x16x32_bf16 v[152:155], v[216:219], v[136:139], v[152:155]\n"
      "ds_read_b64_tr_b16 v[216:217], %2 offset:32768\n"
      "ds_read_b64_tr_b16 v[218:219], %2 offset:33792\n"
      "v_mfma_f32_16x16x32_bf16 v[164:167], v[220:223], v[136:139], v[164:167]\n"
      "v_exp_f32_e32 v189, v189\n"
      "v_mfma_f32_16x16x32_bf16 v[160:163], v[220:223], v[120:123], v[160:163]\n"
      "v_add_f32_e32 v233, v233, v186\n"
      "v_add_f32_e32 v233, v233, v187\n"
      "v_mfma_f32_16x16x32_bf16 v[156:159], v[220:223], v[104:107], v[156:159]\n"
      "ds_read_b64_tr_b16 v[220:221], %3 offset:32768\n"
      "ds_read_b64_tr_b16 v[222:223], %3 offset:33792\n"
      "s_waitcnt lgkmcnt(4)\n"
      "v_mfma_f32_16x16x32_bf16 v[144:147], v[224:227], v[108:111], v[144:147]\n"
      "v_exp_f32_e32 v190, v190\n"
      "v_cvt_pk_bf16_f32 v202, v188, v189\n"
      "v_mfma_f32_16x16x32_bf16 v[148:151], v[224:227], v[124:127], v[148:151]\n"
      "v_add_f32_e32 v234, v234, v188\n"
      "v_exp_f32_e32 v191, v191\n"
      "v_mfma_f32_16x16x32_bf16 v[152:155], v[224:227], v[140:143], v[152:155]\n"
      "ds_read_b64_tr_b16 v[224:225], %2 offset:33024\n"
      "ds_read_b64_tr_b16 v[226:227], %2 offset:34048\n"
      "v_mfma_f32_16x16x32_bf16 v[164:167], v[228:231], v[140:143], v[164:167]\n"
      "v_add_f32_e32 v234, v234, v189\n"
      "v_cvt_pk_bf16_f32 v203, v190, v191\n"
      "v_mfma_f32_16x16x32_bf16 v[160:163], v[228:231], v[124:127], v[160:163]\n"
      "v_add_f32_e32 v234, v234, v190\n"
      "v_add_f32_e32 v234, v234, v191\n"
      "v_mfma_f32_16x16x32_bf16 v[156:159], v[228:231], v[108:111], v[156:159]\n"
      "ds_read_b64_tr_b16 v[228:229], %3 offset:33024\n"
      "ds_read_b64_tr_b16 v[230:231], %3 offset:34048\n"
      "s_waitcnt lgkmcnt(4)\n"
      "v_mfma_f32_16x16x32_bf16 v[0:3], v[216:219], v[192:195], v[0:3]\n"
      "v_exp_f32_e32 v144, v144\n"
      "v_mfma_f32_16x16x32_bf16 v[32:35], v[216:219], v[196:199], v[32:35]\n"
      "v_exp_f32_e32 v145, v145\n"
      "v_add_f32_e32 v232, v232, v144\n"
      "v_mfma_f32_16x16x32_bf16 v[64:67], v[216:219], v[200:203], v[64:67]\n"
      "ds_read_b64_tr_b16 v[216:217], %2 offset:33280\n"
      "ds_read_b64_tr_b16 v[218:219], %2 offset:34304\n"
      "v_mfma_f32_16x16x32_bf16 v[68:71], v[220:223], v[200:203], v[68:71]\n"
      "v_add_f32_e32 v232, v232, v145\n"
      "v_exp_f32_e32 v146, v146\n"
      "v_mfma_f32_16x16x32_bf16 v[36:39], v[220:223], v[196:199], v[36:39]\n"
      "v_exp_f32_e32 v147, v147\n"
      "v_mfma_f32_16x16x32_bf16 v[4:7], v[220:223], v[192:195], v[4:7]\n"
      "ds_read_b64_tr_b16 v[220:221], %3 offset:33280\n"
      "ds_read_b64_tr_b16 v[222:223], %3 offset:34304\n"
      "s_waitcnt lgkmcnt(4)\n"
      "v_mfma_f32_16x16x32_bf16 v[8:11], v[224:227], v[192:195], v[8:11]\n"
      "v_add_f32_e32 v232, v232, v146\n"
      "v_add_f32_e32 v232, v232, v147\n"
      "v_mfma_f32_16x16x32_bf16 v[40:43], v[224:227], v[196:199], v[40:43]\n"
      "v_exp_f32_e32 v148, v148\n"
      "v_mfma_f32_16x16x32_bf16 v[72:75], v[224:227], v[200:203], v[72:75]\n"
      "ds_read_b64_tr_b16 v[224:225], %2 offset:33536\n"
      "ds_read_b64_tr_b16 v[226:227], %2 offset:34560\n"
      "v_mfma_f32_16x16x32_bf16 v[76:79], v[228:231], v[200:203], v[76:79]\n"
      "v_exp_f32_e32 v149, v149\n"
      "v_add_f32_e32 v233, v233, v148\n"
      "v_add_f32_e32 v233, v233, v149\n"
      "v_mfma_f32_16x16x32_bf16 v[44:47], v[228:231], v[196:199], v[44:47]\n"
      "v_exp_f32_e32 v150, v150\n"
      "v_mfma_f32_16x16x32_bf16 v[12:15], v[228:231], v[192:195], v[12:15]\n"
      "ds_read_b64_tr_b16 v[228:229], %3 offset:33536\n"
      "ds_read_b64_tr_b16 v[230:231], %3 offset:34560\n"
      "s_waitcnt lgkmcnt(4)\n"
      "v_mfma_f32_16x16x32_bf16 v[16:19], v[216:219], v[192:195], v[16:19]\n"
      "v_exp_f32_e32 v151, v151\n"
      "v_mfma_f32_16x16x32_bf16 v[48:51], v[216:219], v[196:199], v[48:51]\n"
      "v_add_f32_e32 v233, v233, v150\n"
      "v_add_f32_e32 v233, v233, v151\n"
      "v_mfma_f32_16x16x32_bf16 v[80:83], v[216:219], v[200:203], v[80:83]\n"
      "v_exp_f32_e32 v152, v152\n"
      "v_mfma_f32_16x16x32_bf16 v[84:87], v[220:223], v[200:203], v[84:87]\n"
      "s_waitcnt lgkmcnt(0)\n"
      "s_barrier\n"
      "ds_read_b128 v[216:219], %1 offset:49152\n"
      "v_mfma_f32_16x16x32_bf16 v[52:55], v[220:223], v[196:199], v[52:55]\n"
      "v_exp_f32_e32 v153, v153\n"
      "v_add_f32_e32 v234, v234, v152\n"
      "v_add_f32_e32 v234, v234, v153\n"
      "v_mfma_f32_16x16x32_bf16 v[20:23], v[220:223], v[192:195], v[20:23]\n"
      "ds_read_b128 v[220:223], %1 offset:50176\n"
      "v_mfma_f32_16x16x32_bf16 v[24:27], v[224:227], v[192:195], v[24:27]\n"
      "v_exp_f32_e32 v154, v154\n"
      "v_mfma_f32_16x16x32_bf16 v[56:59], v[224:227], v[196:199], v[56:59]\n"
      "v_exp_f32_e32 v155, v155\n"
      "v_add_f32_e32 v234, v234, v154\n"
      "v_add_f32_e32 v234, v234, v155\n"
      "v_mfma_f32_16x16x32_bf16 v[88:91], v[224:227], v[200:203], v[88:91]\n"
      "ds_read_b128 v[224:227], %1 offset:49408\n"
      "v_mfma_f32_16x16x32_bf16 v[92:95], v[228:231], v[200:203], v[92:95]\n"
      "v_exp_f32_e32 v156, v156\n"
      "v_exp_f32_e32 v157, v157\n"
      "v_mfma_f32_16x16x32_bf16 v[60:63], v[228:231], v[196:199], v[60:63]\n"
      "v_add_f32_e32 v232, v232, v156\n"
      "v_add_f32_e32 v232, v232, v157\n"
      "v_mfma_f32_16x16x32_bf16 v[28:31], v[228:231], v[192:195], v[28:31]\n"
      "ds_read_b128 v[228:231], %1 offset:50432\n"
      "s_waitcnt lgkmcnt(2)\n"
      "v_mfma_f32_16x16x32_bf16 v[168:171], v[216:219], v[96:99], v[204:207]\n"
      "v_exp_f32_e32 v158, v158\n"
      "v_mfma_f32_16x16x32_bf16 v[172:175], v[216:219], v[112:115], v[208:211]\n"
      "v_cvt_pk_bf16_f32 v192, v144, v145\n"
      "v_cvt_pk_bf16_f32 v193, v146, v147\n"
      "v_mfma_f32_16x16x32_bf16 v[176:179], v[216:219], v[128:131], v[212:215]\n"
      "ds_read_b128 v[216:219], %1 offset:49664\n"
      "v_exp_f32_e32 v159, v159\n"
      "v_mfma_f32_16x16x32_bf16 v[188:191], v[220:223], v[128:131], v[212:215]\n"
      "v_cvt_pk_bf16_f32 v196, v148, v149\n"
      "v_cvt_pk_bf16_f32 v197, v150, v151\n"
      "v_mfma_f32_16x16x32_bf16 v[184:187], v[220:223], v[112:115], v[208:211]\n"
      "v_exp_f32_e32 v160, v160\n"
      "v_cvt_pk_bf16_f32 v200, v152, v153\n"
      "v_mfma_f32_16x16x32_bf16 v[180:183], v[220:223], v[96:99], v[204:207]\n"
      "ds_read_b128 v[220:223], %1 offset:50688\n"
      "s_waitcnt lgkmcnt(2)\n"
      "v_mfma_f32_16x16x32_bf16 v[168:171], v[224:227], v[100:103], v[168:171]\n"
      "v_cvt_pk_bf16_f32 v201, v154, v155\n"
      "v_exp_f32_e32 v161, v161\n"
      "v_mfma_f32_16x16x32_bf16 v[172:175], v[224:227], v[116:119], v[172:175]\n"
      "v_cvt_pk_bf16_f32 v194, v156, v157\n"
      "v_cvt_pk_bf16_f32 v195, v158, v159\n"
      "v_mfma_f32_16x16x32_bf16 v[176:179], v[224:227], v[132:135], v[176:179]\n"
      "ds_read_b128 v[224:227], %1 offset:49920\n"
      "v_exp_f32_e32 v162, v162\n"
      "v_mfma_f32_16x16x32_bf16 v[188:191], v[228:231], v[132:135], v[188:191]\n"
      "v_add_f32_e32 v232, v232, v158\n"
      "v_add_f32_e32 v232, v232, v159\n"
      "v_mfma_f32_16x16x32_bf16 v[184:187], v[228:231], v[116:119], v[184:187]\n"
      "v_exp_f32_e32 v163, v163\n"
      "v_mfma_f32_16x16x32_bf16 v[180:183], v[228:231], v[100:103], v[180:183]\n"
      "ds_read_b128 v[228:231], %1 offset:50944\n"
      "v_cvt_pk_bf16_f32 v198, v160, v161\n"
      "s_waitcnt lgkmcnt(2)\n"
      "v_mfma_f32_16x16x32_bf16 v[168:171], v[216:219], v[104:107], v[168:171]\n"
      "v_add_f32_e32 v233, v233, v160\n"
      "v_mfma_f32_16x16x32_bf16 v[172:175], v[216:219], v[120:123], v[172:175]\n"
      "v_exp_f32_e32 v164, v164\n"
      "v_add_f32_e32 v233, v233, v161\n"
      "v_cvt_pk_bf16_f32 v199, v162, v163\n"
      "v_mfma_f32_16x16x32_bf16 v[176:179], v[216:219], v[136:139], v[176:179]\n"
      "ds_read_b64_tr_b16 v[216:217], %2 offset:49152\n"
      "ds_read_b64_tr_b16 v[218:219], %2 offset:50176\n"
      "v_mfma_f32_16x16x32_bf16 v[188:191], v[220:223], v[136:139], v[188:191]\n"
      "v_exp_f32_e32 v165, v165\n"
      "v_mfma_f32_16x16x32_bf16 v[184:187], v[220:223], v[120:123], v[184:187]\n"
      "v_add_f32_e32 v233, v233, v162\n"
      "v_add_f32_e32 v233, v233, v163\n"
      "v_mfma_f32_16x16x32_bf16 v[180:183], v[220:223], v[104:107], v[180:183]\n"
      "ds_read_b64_tr_b16 v[220:221], %3 offset:49152\n"
      "ds_read_b64_tr_b16 v[222:223], %3 offset:50176\n"
      "s_waitcnt lgkmcnt(4)\n"
      "v_mfma_f32_16x16x32_bf16 v[168:171], v[224:227], v[108:111], v[168:171]\n"
      "v_exp_f32_e32 v166, v166\n"
      "v_cvt_pk_bf16_f32 v202, v164, v165\n"
      "v_mfma_f32_16x16x32_bf16 v[172:175], v[224:227], v[124:127], v[172:175]\n"
      "v_add_f32_e32 v234, v234, v164\n"
      "v_exp_f32_e32 v167, v167\n"
      "v_mfma_f32_16x16x32_bf16 v[176:179], v[224:227], v[140:143], v[176:179]\n"
      "ds_read_b64_tr_b16 v[224:225], %2 offset:49408\n"
      "ds_read_b64_tr_b16 v[226:227], %2 offset:50432\n"
      "v_mfma_f32_16x16x32_bf16 v[188:191], v[228:231], v[140:143], v[188:191]\n"
      "v_add_f32_e32 v234, v234, v165\n"
      "v_cvt_pk_bf16_f32 v203, v166, v167\n"
      "v_mfma_f32_16x16x32_bf16 v[184:187], v[228:231], v[124:127], v[184:187]\n"
      "v_add_f32_e32 v234, v234, v166\n"
      "v_add_f32_e32 v234, v234, v167\n"
      "v_mfma_f32_16x16x32_bf16 v[180:183], v[228:231], v[108:111], v[180:183]\n"
      "ds_read_b64_tr_b16 v[228:229], %3 offset:49408\n"
      "ds_read_b64_tr_b16 v[230:231], %3 offset:50432\n"
      "s_waitcnt lgkmcnt(4)\n"
      "v_mfma_f32_16x16x32_bf16 v[0:3], v[216:219], v[192:195], v[0:3]\n"
      "ds_read_b128 v[96:99], %4 offset:0\n"
      "v_exp_f32_e32 v168, v168\n"
      "v_mfma_f32_16x16x32_bf16 v[32:35], v[216:219], v[196:199], v[32:35]\n"
      "ds_read_b128 v[112:115], %4 offset:4096\n"
      "v_exp_f32_e32 v169, v169\n"
      "v_add_f32_e32 v232, v232, v168\n"
      "v_mfma_f32_16x16x32_bf16 v[64:67], v[216:219], v[200:203], v[64:67]\n"
      "ds_read_b64_tr_b16 v[216:217], %2 offset:49664\n"
      "ds_read_b64_tr_b16 v[218:219], %2 offset:50688\n"
      "ds_read_b128 v[128:131], %4 offset:8192\n"
      "v_mfma_f32_16x16x32_bf16 v[68:71], v[220:223], v[200:203], v[68:71]\n"
      "ds_read_b128 v[100:103], %4 offset:256\n"
      "v_add_f32_e32 v232, v232, v169\n"
      "v_exp_f32_e32 v170, v170\n"
      "v_mfma_f32_16x16x32_bf16 v[36:39], v[220:223], v[196:199], v[36:39]\n"
      "ds_read_b128 v[116:119], %4 offset:4352\n"
      "v_exp_f32_e32 v171, v171\n"
      "v_mfma_f32_16x16x32_bf16 v[4:7], v[220:223], v[192:195], v[4:7]\n"
      "ds_read_b64_tr_b16 v[220:221], %3 offset:49664\n"
      "ds_read_b64_tr_b16 v[222:223], %3 offset:50688\n"
      "ds_read_b128 v[132:135], %4 offset:8448\n"
      "s_waitcnt lgkmcnt(10)\n"
      "v_mfma_f32_16x16x32_bf16 v[8:11], v[224:227], v[192:195], v[8:11]\n"
      "ds_read_b128 v[104:107], %4 offset:512\n"
      "v_add_f32_e32 v232, v232, v170\n"
      "v_add_f32_e32 v232, v232, v171\n"
      "v_mfma_f32_16x16x32_bf16 v[40:43], v[224:227], v[196:199], v[40:43]\n"
      "ds_read_b128 v[120:123], %4 offset:4608\n"
      "ds_read_b128 v[136:139], %4 offset:8704\n"
      "v_exp_f32_e32 v172, v172\n"
      "v_mfma_f32_16x16x32_bf16 v[72:75], v[224:227], v[200:203], v[72:75]\n"
      "ds_read_b64_tr_b16 v[224:225], %2 offset:49920\n"
      "ds_read_b64_tr_b16 v[226:227], %2 offset:50944\n"
      "ds_read_b128 v[108:111], %4 offset:768\n"
      "v_mfma_f32_16x16x32_bf16 v[76:79], v[228:231], v[200:203], v[76:79]\n"
      "v_exp_f32_e32 v173, v173\n"
      "v_add_f32_e32 v233, v233, v172\n"
      "v_add_f32_e32 v233, v233, v173\n"
      "v_mfma_f32_16x16x32_bf16 v[44:47], v[228:231], v[196:199], v[44:47]\n"
      "ds_read_b128 v[124:127], %4 offset:4864\n"
      "ds_read_b128 v[140:143], %4 offset:8960\n"
      "v_exp_f32_e32 v174, v174\n"
      "v_mfma_f32_16x16x32_bf16 v[12:15], v[228:231], v[192:195], v[12:15]\n"
      "ds_read_b64_tr_b16 v[228:229], %3 offset:49920\n"
      "ds_read_b64_tr_b16 v[230:231], %3 offset:50944\n"
      "s_waitcnt lgkmcnt(11)\n"
      "v_mfma_f32_16x16x32_bf16 v[16:19], v[216:219], v[192:195], v[16:19]\n"
      "v_exp_f32_e32 v175, v175\n"
      "v_mfma_f32_16x16x32_bf16 v[48:51], v[216:219], v[196:199], v[48:51]\n"
      "v_add_f32_e32 v233, v233, v174\n"
      "v_add_f32_e32 v233, v233, v175\n"
      "v_mfma_f32_16x16x32_bf16 v[80:83], v[216:219], v[200:203], v[80:83]\n"
      "v_exp_f32_e32 v176, v176\n"
      "v_mfma_f32_16x16x32_bf16 v[84:87], v[220:223], v[200:203], v[84:87]\n"
      "s_waitcnt lgkmcnt(0)\n"
      "s_barrier\n"
      "ds_read_b128 v[216:219], %1 offset:0\n"
      "v_mfma_f32_16x16x32_bf16 v[52:55], v[220:223], v[196:199], v[52:55]\n"
      "v_exp_f32_e32 v177, v177\n"
      "v_add_f32_e32 v234, v234, v176\n"
      "v_add_f32_e32 v234, v234, v177\n"
      "v_mfma_f32_16x16x32_bf16 v[20:23], v[220:223], v[192:195], v[20:23]\n"
      "ds_read_b128 v[220:223], %1 offset:1024\n"
      "v_mfma_f32_16x16x32_bf16 v[24:27], v[224:227], v[192:195], v[24:27]\n"
      "v_exp_f32_e32 v178, v178\n"
      "v_mfma_f32_16x16x32_bf16 v[56:59], v[224:227], v[196:199], v[56:59]\n"
      "v_exp_f32_e32 v179, v179\n"
      "v_add_f32_e32 v234, v234, v178\n"
      "v_add_f32_e32 v234, v234, v179\n"
      "v_mfma_f32_16x16x32_bf16 v[88:91], v[224:227], v[200:203], v[88:91]\n"
      "ds_read_b128 v[224:227], %1 offset:256\n"
      "v_mfma_f32_16x16x32_bf16 v[92:95], v[228:231], v[200:203], v[92:95]\n"
      "v_exp_f32_e32 v180, v180\n"
      "v_exp_f32_e32 v181, v181\n"
      "v_mfma_f32_16x16x32_bf16 v[60:63], v[228:231], v[196:199], v[60:63]\n"
      "v_add_f32_e32 v232, v232, v180\n"
      "v_add_f32_e32 v232, v232, v181\n"
      "v_mfma_f32_16x16x32_bf16 v[28:31], v[228:231], v[192:195], v[28:31]\n"
      "ds_read_b128 v[228:231], %1 offset:1280\n"
      "s_sub_u32 s40, s40, 1\n"
      "s_cmp_eq_u32 s40, 0\n"
      "s_cbranch_scc1 LTAIL%=\n"
      "s_waitcnt lgkmcnt(2)\n"
      "v_mfma_f32_16x16x32_bf16 v[144:147], v[216:219], v[96:99], 0\n"
      "v_exp_f32_e32 v182, v182\n"
      "v_mfma_f32_16x16x32_bf16 v[148:151], v[216:219], v[112:115], 0\n"
      "v_cvt_pk_bf16_f32 v192, v168, v169\n"
      "v_cvt_pk_bf16_f32 v193, v170, v171\n"
      "v_mfma_f32_16x16x32_bf16 v[152:155], v[216:219], v[128:131], 0\n"
      "ds_read_b128 v[216:219], %1 offset:512\n"
      "v_exp_f32_e32 v183, v183\n"
      "v_mfma_f32_16x16x32_bf16 v[164:167], v[220:223], v[128:131], 0\n"
      "v_cvt_pk_bf16_f32 v196, v172, v173\n"
      "v_cvt_pk_bf16_f32 v197, v174, v175\n"
      "v_mfma_f32_16x16x32_bf16 v[160:163], v[220:223], v[112:115], 0\n"
      "v_exp_f32_e32 v184, v184\n"
      "v_cvt_pk_bf16_f32 v200, v176, v177\n"
      "v_mfma_f32_16x16x32_bf16 v[156:159], v[220:223], v[96:99], 0\n"
      "ds_read_b128 v[220:223], %1 offset:1536\n"
      "s_waitcnt lgkmcnt(2)\n"
      "v_mfma_f32_16x16x32_bf16 v[144:147], v[224:227], v[100:103], v[144:147]\n"
      "v_cvt_pk_bf16_f32 v201, v178, v179\n"
      "v_exp_f32_e32 v185, v185\n"
      "v_mfma_f32_16x16x32_bf16 v[148:151], v[224:227], v[116:119], v[148:151]\n"
      "v_cvt_pk_bf16_f32 v194, v180, v181\n"
      "v_cvt_pk_bf16_f32 v195, v182, v183\n"
      "v_mfma_f32_16x16x32_bf16 v[152:155], v[224:227], v[132:135], v[152:155]\n"
      "ds_read_b128 v[224:227], %1 offset:768\n"
      "v_exp_f32_e32 v186, v186\n"
      "v_mfma_f32_16x16x32_bf16 v[164:167], v[228:231], v[132:135], v[164:167]\n"
      "v_add_f32_e32 v232, v232, v182\n"
      "v_add_f32_e32 v232, v232, v183\n"
      "v_mfma_f32_16x16x32_bf16 v[160:163], v[228:231], v[116:119], v[160:163]\n"
      "v_exp_f32_e32 v187, v187\n"
      "v_mfma_f32_16x16x32_bf16 v[156:159], v[228:231], v[100:103], v[156:159]\n"
      "ds_read_b128 v[228:231], %1 offset:1792\n"
      "v_cvt_pk_bf16_f32 v198, v184, v185\n"
      "s_waitcnt lgkmcnt(2)\n"
      "v_mfma_f32_16x16x32_bf16 v[144:147], v[216:219], v[104:107], v[144:147]\n"
      "v_add_f32_e32 v233, v233, v184\n"
      "v_mfma_f32_16x16x32_bf16 v[148:151], v[216:219], v[120:123], v[148:151]\n"
      "v_exp_f32_e32 v188, v188\n"
      "v_add_f32_e32 v233, v233, v185\n"
      "v_cvt_pk_bf16_f32 v199, v186, v187\n"
      "v_mfma_f32_16x16x32_bf16 v[152:155], v[216:219], v[136:139], v[152:155]\n"
      "ds_read_b64_tr_b16 v[216:217], %2 offset:0\n"
      "ds_read_b64_tr_b16 v[218:219], %2 offset:1024\n"
      "v_mfma_f32_16x16x32_bf16 v[164:167], v[220:223], v[136:139], v[164:167]\n"
      "v_exp_f32_e32 v189, v189\n"
      "v_mfma_f32_16x16x32_bf16 v[160:163], v[220:223], v[120:123], v[160:163]\n"
      "v_add_f32_e32 v233, v233, v186\n"
      "v_add_f32_e32 v233, v233, v187\n"
      "v_mfma_f32_16x16x32_bf16 v[156:159], v[220:223], v[104:107], v[156:159]\n"
      "ds_read_b64_tr_b16 v[220:221], %3 offset:0\n"
      "ds_read_b64_tr_b16 v[222:223], %3 offset:1024\n"
      "s_waitcnt lgkmcnt(4)\n"
      "v_mfma_f32_16x16x32_bf16 v[144:147], v[224:227], v[108:111], v[144:147]\n"
      "v_exp_f32_e32 v190, v190\n"
      "v_cvt_pk_bf16_f32 v202, v188, v189\n"
      "v_mfma_f32_16x16x32_bf16 v[148:151], v[224:227], v[124:127], v[148:151]\n"
      "v_add_f32_e32 v234, v234, v188\n"
      "v_exp_f32_e32 v191, v191\n"
      "v_mfma_f32_16x16x32_bf16 v[152:155], v[224:227], v[140:143], v[152:155]\n"
      "ds_read_b64_tr_b16 v[224:225], %2 offset:256\n"
      "ds_read_b64_tr_b16 v[226:227], %2 offset:1280\n"
      "v_mfma_f32_16x16x32_bf16 v[164:167], v[228:231], v[140:143], v[164:167]\n"
      "v_add_f32_e32 v234, v234, v189\n"
      "v_cvt_pk_bf16_f32 v203, v190, v191\n"
      "v_mfma_f32_16x16x32_bf16 v[160:163], v[228:231], v[124:127], v[160:163]\n"
      "v_add_f32_e32 v234, v234, v190\n"
      "v_add_f32_e32 v234, v234, v191\n"
      "v_mfma_f32_16x16x32_bf16 v[156:159], v[228:231], v[108:111], v[156:159]\n"
      "ds_read_b64_tr_b16 v[228:229], %3 offset:256\n"
      "ds_read_b64_tr_b16 v[230:231], %3 offset:1280\n"
      "s_waitcnt lgkmcnt(4)\n"
      "v_mfma_f32_16x16x32_bf16 v[0:3], v[216:219], v[192:195], v[0:3]\n"
      "v_mov_b32_e32 v206, v232\n"
      "v_mov_b32_e32 v210, v233\n"
      "v_mov_b32_e32 v214, v234\n"
      "v_permlane16_swap_b32_e32 v232, v206\n"
      "v_permlane16_swap_b32_e32 v233, v210\n"
      "v_permlane16_swap_b32_e32 v234, v214\n"
      "v_add_f32_e32 v232, v232, v206\n"
      "v_mfma_f32_16x16x32_bf16 v[32:35], v[216:219], v[196:199], v[32:35]\n"
      "v_add_f32_e32 v233, v233, v210\n"
      "v_add_f32_e32 v234, v234, v214\n"
      "v_mov_b32_e32 v206, v232\n"
      "v_mov_b32_e32 v210, v233\n"
      "v_mov_b32_e32 v214, v234\n"
      "v_permlane32_swap_b32_e32 v232, v206\n"
      "v_mfma_f32_16x16x32_bf16 v[64:67], v[216:219], v[200:203], v[64:67]\n"
      "ds_read_b64_tr_b16 v[216:217], %2 offset:512\n"
      "ds_read_b64_tr_b16 v[218:219], %2 offset:1536\n"
      "v_permlane32_swap_b32_e32 v233, v210\n"
      "v_permlane32_swap_b32_e32 v234, v214\n"
      "v_add_f32_e32 v232, v232, v206\n"
      "v_add_f32_e32 v233, v233, v210\n"
      "v_add_f32_e32 v234, v234, v214\n"
      "v_mfma_f32_16x16x32_bf16 v[68:71], v[220:223], v[200:203], v[68:71]\n"
      "v_rcp_f32_e32 v235, v232\n"
      "v_rcp_f32_e32 v236, v233\n"
      "v_rcp_f32_e32 v237, v234\n"
      "v_mfma_f32_16x16x32_bf16 v[36:39], v[220:223], v[196:199], v[36:39]\n"
      "v_add_f32_e32 v206, v232, v233\n"
      "v_add_f32_e32 v206, v206, v234\n"
      "v_cmp_ngt_f32_e32 vcc, s44, v206\n"
      "v_xor_b32_e32 v238, 32, %5\n"
      "v_xor_b32_e32 v239, 64, %5\n"
      "v_xor_b32_e32 v240, 96, %5\n"
      "v_mfma_f32_16x16x32_bf16 v[4:7], v[220:223], v[192:195], v[4:7]\n"
      "ds_read_b64_tr_b16 v[220:221], %3 offset:512\n"
      "ds_read_b64_tr_b16 v[222:223], %3 offset:1536\n"
      "s_cmp_lg_u64 vcc, 0\n"
      "s_cselect_b32 s42, s43, 0\n"
      "s_or_b32 %0, %0, s42\n"
      "s_lshl_b32 s43, s43, 1\n"
      "s_waitcnt lgkmcnt(4)\n"
      "v_mfma_f32_16x16x32_bf16 v[8:11], v[224:227], v[192:195], v[8:11]\n"
      "v_max3_f32 v204, v144, v145, v146\n"
      "v_max3_f32 v208, v148, v149, v150\n"
      "v_max3_f32 v212, v152, v153, v154\n"
      "v_max3_f32 v205, v147, v156, v157\n"
      "v_max3_f32 v204, v204, v158, v159\n"
      "v_max3_f32 v209, v151, v160, v161\n"
      "v_max3_f32 v208, v208, v162, v163\n"
      "v_mfma_f32_16x16x32_bf16 v[40:43], v[224:227], v[196:199], v[40:43]\n"
      "v_max3_f32 v213, v155, v164, v165\n"
      "v_max3_f32 v212, v212, v166, v167\n"
      "v_max_f32_e32 v204, v204, v205\n"
      "v_max_f32_e32 v208, v208, v209\n"
      "v_max_f32_e32 v212, v212, v213\n"
      "v_mov_b32_e32 v205, v204\n"
      "v_mfma_f32_16x16x32_bf16 v[72:75], v[224:227], v[200:203], v[72:75]\n"
      "ds_read_b64_tr_b16 v[224:225], %2 offset:768\n"
      "ds_read_b64_tr_b16 v[226:227], %2 offset:1792\n"
      "v_mov_b32_e32 v209, v208\n"
      "v_mov_b32_e32 v213, v212\n"
      "v_permlane16_swap_b32_e32 v204, v205\n"
      "v_permlane16_swap_b32_e32 v208, v209\n"
      "v_permlane16_swap_b32_e32 v212, v213\n"
      "v_mfma_f32_16x16x32_bf16 v[76:79], v[228:231], v[200:203], v[76:79]\n"
      "v_max_f32_e32 v204, v204, v205\n"
      "v_max_f32_e32 v208, v208, v209\n"
      "v_max_f32_e32 v212, v212, v213\n"
      "v_mov_b32_e32 v205, v204\n"
      "v_mov_b32_e32 v209, v208\n"
      "v_mov_b32_e32 v213, v212\n"
      "v_mfma_f32_16x16x32_bf16 v[44:47], v[228:231], v[196:199], v[44:47]\n"
      "v_permlane32_swap_b32_e32 v204, v205\n"
      "v_permlane32_swap_b32_e32 v208, v209\n"
      "v_permlane32_swap_b32_e32 v212, v213\n"
      "v_max_f32_e32 v204, v204, v205\n"
      "v_max_f32_e32 v208, v208, v209\n"
      "v_max_f32_e32 v212, v212, v213\n"
      "v_sub_f32_e32 v144, v144, v204\n"
      "v_mfma_f32_16x16x32_bf16 v[12:15], v[228:231], v[192:195], v[12:15]\n"
      "ds_read_b64_tr_b16 v[228:229], %3 offset:768\n"
      "ds_read_b64_tr_b16 v[230:231], %3 offset:1792\n"
      "v_sub_f32_e32 v145, v145, v204\n"
      "v_sub_f32_e32 v146, v146, v204\n"
      "v_sub_f32_e32 v147, v147, v204\n"
      "v_sub_f32_e32 v148, v148, v208\n"
      "s_waitcnt lgkmcnt(4)\n"
      "v_mfma_f32_16x16x32_bf16 v[16:19], v[216:219], v[192:195], v[16:19]\n"
      "v_sub_f32_e32 v149, v149, v208\n"
      "v_sub_f32_e32 v150, v150, v208\n"
      "v_sub_f32_e32 v151, v151, v208\n"
      "v_sub_f32_e32 v152, v152, v212\n"
      "v_sub_f32_e32 v153, v153, v212\n"
      "v_sub_f32_e32 v154, v154, v212\n"
      "v_mfma_f32_16x16x32_bf16 v[48:51], v[216:219], v[196:199], v[48:51]\n"
      "v_sub_f32_e32 v155, v155, v212\n"
      "v_sub_f32_e32 v156, v156, v204\n"
      "v_sub_f32_e32 v157, v157, v204\n"
      "v_sub_f32_e32 v158, v158, v204\n"
      "v_sub_f32_e32 v159, v159, v204\n"
      "v_sub_f32_e32 v160, v160, v208\n"
      "v_sub_f32_e32 v161, v161, v208\n"
      "v_mfma_f32_16x16x32_bf16 v[80:83], v[216:219], v[200:203], v[80:83]\n"
      "v_sub_f32_e32 v162, v162, v208\n"
      "v_sub_f32_e32 v163, v163, v208\n"
      "v_sub_f32_e32 v164, v164, v212\n"
      "v_sub_f32_e32 v165, v165, v212\n"
      "v_sub_f32_e32 v166, v166, v212\n"
      "v_sub_f32_e32 v167, v167, v212\n"
      "v_mfma_f32_16x16x32_bf16 v[84:87], v[220:223], v[200:203], v[84:87]\n"
      "v_sub_f32_e32 v207, 0, v204\n"
      "v_sub_f32_e32 v206, 0, v204\n"
      "v_sub_f32_e32 v205, 0, v204\n"
      "v_sub_f32_e32 v204, 0, v204\n"
      "s_waitcnt lgkmcnt(0)\n"
      "s_barrier\n"
      "ds_read_b128 v[216:219], %1 offset:16384\n"
      "v_mfma_f32_16x16x32_bf16 v[52:55], v[220:223], v[196:199], v[52:55]\n"
      "v_sub_f32_e32 v211, 0, v208\n"
      "v_sub_f32_e32 v210, 0, v208\n"
      "v_sub_f32_e32 v209, 0, v208\n"
      "v_sub_f32_e32 v208, 0, v208\n"
      "v_sub_f32_e32 v215, 0, v212\n"
      "v_sub_f32_e32 v214, 0, v212\n"
      "v_mfma_f32_16x16x32_bf16 v[20:23], v[220:223], v[192:195], v[20:23]\n"
      "ds_read_b128 v[220:223], %1 offset:17408\n"
      "v_sub_f32_e32 v213, 0, v212\n"
      "v_sub_f32_e32 v212, 0, v212\n"
      "v_exp_f32_e32 v144, v144\n"
      "v_exp_f32_e32 v145, v145\n"
      "v_mfma_f32_16x16x32_bf16 v[24:27], v[224:227], v[192:195], v[24:27]\n"
      "v_add_f32_e32 v232, v144, v145\n"
      "v_exp_f32_e32 v146, v146\n"
      "v_exp_f32_e32 v147, v147\n"
      "v_add_f32_e32 v232, v232, v146\n"
      "v_mfma_f32_16x16x32_bf16 v[56:59], v[224:227], v[196:199], v[56:59]\n"
      "v_add_f32_e32 v232, v232, v147\n"
      "v_exp_f32_e32 v148, v148\n"
      "v_exp_f32_e32 v149, v149\n"
      "s_nop 0\n"
      "v_add_f32_e32 v233, v148, v149\n"
      "v_mfma_f32_16x16x32_bf16 v[88:91], v[224:227], v[200:203], v[88:91]\n"
      "ds_read_b128 v[224:227], %1 offset:16640\n"
      "v_exp_f32_e32 v150, v150\n"
      "v_exp_f32_e32 v151, v151\n"
      "v_add_f32_e32 v233, v233, v150\n"
      "v_add_f32_e32 v233, v233, v151\n"
      "v_mfma_f32_16x16x32_bf16 v[92:95], v[228:231], v[200:203], v[92:95]\n"
      "v_exp_f32_e32 v152, v152\n"
      "v_exp_f32_e32 v153, v153\n"
      "s_nop 0\n"
      "v_add_f32_e32 v234, v152, v153\n"
      "v_exp_f32_e32 v154, v154\n"
      "v_mfma_f32_16x16x32_bf16 v[60:63], v[228:231], v[196:199], v[60:63]\n"
      "v_exp_f32_e32 v155, v155\n"
      "v_add_f32_e32 v234, v234, v154\n"
      "v_add_f32_e32 v234, v234, v155\n"
      "v_exp_f32_e32 v156, v156\n"
      "v_mfma_f32_16x16x32_bf16 v[28:31], v[228:231], v[192:195], v[28:31]\n"
      "ds_read_b128 v[228:231], %1 offset:17664\n"
      "v_exp_f32_e32 v157, v157\n"
      "v_add_f32_e32 v232, v232, v156\n"
      "v_add_f32_e32 v232, v232, v157\n"
      "s_branch LHEAD%=\n"
      "LTAIL%=:\n"
      "ds_read_b64_tr_b16 v[216:217], %2 offset:0\n"
      "ds_read_b64_tr_b16 v[218:219], %2 offset:1024\n"
      "ds_read_b64_tr_b16 v[220:221], %3 offset:0\n"
      "ds_read_b64_tr_b16 v[222:223], %3 offset:1024\n"
      "ds_read_b64_tr_b16 v[224:225], %2 offset:256\n"
      "ds_read_b64_tr_b16 v[226:227], %2 offset:1280\n"
      "ds_read_b64_tr_b16 v[228:229], %3 offset:256\n"
      "ds_read_b64_tr_b16 v[230:231], %3 offset:1280\n"
      "v_exp_f32_e32 v182, v182\n"
      "v_cvt_pk_bf16_f32 v192, v168, v169\n"
      "v_cvt_pk_bf16_f32 v193, v170, v171\n"
      "v_exp_f32_e32 v183, v183\n"
      "v_cvt_pk_bf16_f32 v196, v172, v173\n"
      "v_cvt_pk_bf16_f32 v197, v174, v175\n"
      "v_exp_f32_e32 v184, v184\n"
      "v_cvt_pk_bf16_f32 v200, v176, v177\n"
      "v_cvt_pk_bf16_f32 v201, v178, v179\n"
      "v_exp_f32_e32 v185, v185\n"
      "v_cvt_pk_bf16_f32 v194, v180, v181\n"
      "v_cvt_pk_bf16_f32 v195, v182, v183\n"
      "v_exp_f32_e32 v186, v186\n"
      "v_add_f32_e32 v232, v232, v182\n"
      "v_add_f32_e32 v232, v232, v183\n"
      "v_exp_f32_e32 v187, v187\n"
      "v_cvt_pk_bf16_f32 v198, v184, v185\n"
      "v_add_f32_e32 v233, v233, v184\n"
      "v_exp_f32_e32 v188, v188\n"
      "v_add_f32_e32 v233, v233, v185\n"
      "v_cvt_pk_bf16_f32 v199, v186, v187\n"
      "v_exp_f32_e32 v189, v189\n"
      "v_add_f32_e32 v233, v233, v186\n"
      "v_add_f32_e32 v233, v233, v187\n"
      "v_exp_f32_e32 v190, v190\n"
      "v_cvt_pk_bf16_f32 v202, v188, v189\n"
      "v_add_f32_e32 v234, v234, v188\n"
      "v_exp_f32_e32 v191, v191\n"
      "v_add_f32_e32 v234, v234, v189\n"
      "v_cvt_pk_bf16_f32 v203, v190, v191\n"
      "v_add_f32_e32 v234, v234, v190\n"
      "v_add_f32_e32 v234, v234, v191\n"
      "s_waitcnt lgkmcnt(4)\n"
      "v_mfma_f32_16x16x32_bf16 v[0:3], v[216:219], v[192:195], v[0:3]\n"
      "v_mov_b32_e32 v206, v232\n"
      "v_mov_b32_e32 v210, v233\n"
      "v_mov_b32_e32 v214, v234\n"
      "v_mfma_f32_16x16x32_bf16 v[32:35], v[216:219], v[196:199], v[32:35]\n"
      "v_permlane16_swap_b32_e32 v232, v206\n"
      "v_permlane16_swap_b32_e32 v233, v210\n"
      "v_permlane16_swap_b32_e32 v234, v214\n"
      "v_add_f32_e32 v232, v232, v206\n"
      "v_mfma_f32_16x16x32_bf16 v[64:67], v[216:219], v[200:203], v[64:67]\n"
      "ds_read_b64_tr_b16 v[216:217], %2 offset:512\n"
      "ds_read_b64_tr_b16 v[218:219], %2 offset:1536\n"
      "v_add_f32_e32 v233, v233, v210\n"
      "v_mfma_f32_16x16x32_bf16 v[68:71], v[220:223], v[200:203], v[68:71]\n"
      "v_add_f32_e32 v234, v234, v214\n"
      "v_mov_b32_e32 v206, v232\n"
      "v_mov_b32_e32 v210, v233\n"
      "v_mov_b32_e32 v214, v234\n"
      "v_mfma_f32_16x16x32_bf16 v[36:39], v[220:223], v[196:199], v[36:39]\n"
      "v_permlane32_swap_b32_e32 v232, v206\n"
      "v_permlane32_swap_b32_e32 v233, v210\n"
      "v_permlane32_swap_b32_e32 v234, v214\n"
      "v_mfma_f32_16x16x32_bf16 v[4:7], v[220:223], v[192:195], v[4:7]\n"
      "ds_read_b64_tr_b16 v[220:221], %3 offset:512\n"
      "ds_read_b64_tr_b16 v[222:223], %3 offset:1536\n"
      "s_waitcnt lgkmcnt(4)\n"
      "v_mfma_f32_16x16x32_bf16 v[8:11], v[224:227], v[192:195], v[8:11]\n"
      "v_add_f32_e32 v232, v232, v206\n"
      "v_add_f32_e32 v233, v233, v210\n"
      "v_add_f32_e32 v234, v234, v214\n"
      "v_mfma_f32_16x16x32_bf16 v[40:43], v[224:227], v[196:199], v[40:43]\n"
      "v_rcp_f32_e32 v235, v232\n"
      "v_rcp_f32_e32 v236, v233\n"
      "v_mfma_f32_16x16x32_bf16 v[72:75], v[224:227], v[200:203], v[72:75]\n"
      "ds_read_b64_tr_b16 v[224:225], %2 offset:768\n"
      "ds_read_b64_tr_b16 v[226:227], %2 offset:1792\n"
      "v_rcp_f32_e32 v237, v234\n"
      "v_mfma_f32_16x16x32_bf16 v[76:79], v[228:231], v[200:203], v[76:79]\n"
      "v_add_f32_e32 v206, v232, v233\n"
      "v_add_f32_e32 v206, v206, v234\n"
      "v_cmp_ngt_f32_e32 vcc, s44, v206\n"
      "v_mfma_f32_16x16x32_bf16 v[44:47], v[228:231], v[196:199], v[44:47]\n"
      "v_xor_b32_e32 v238, 32, %5\n"
      "v_xor_b32_e32 v239, 64, %5\n"
      "v_xor_b32_e32 v240, 96, %5\n"
      "v_mfma_f32_16x16x32_bf16 v[12:15], v[228:231], v[192:195], v[12:15]\n"
      "ds_read_b64_tr_b16 v[228:229], %3 offset:768\n"
      "ds_read_b64_tr_b16 v[230:231], %3 offset:1792\n"
      "s_waitcnt lgkmcnt(4)\n"
      "v_mfma_f32_16x16x32_bf16 v[16:19], v[216:219], v[192:195], v[16:19]\n"
      "s_cmp_lg_u64 vcc, 0\n"
      "s_cselect_b32 s42, s43, 0\n"
      "s_or_b32 %0, %0, s42\n"
      "s_lshl_b32 s43, s43, 1\n"
      "v_mfma_f32_16x16x32_bf16 v[48:51], v[216:219], v[196:199], v[48:51]\n"
      "v_mfma_f32_16x16x32_bf16 v[80:83], v[216:219], v[200:203], v[80:83]\n"
      "v_mfma_f32_16x16x32_bf16 v[84:87], v[220:223], v[200:203], v[84:87]\n"
      "v_mfma_f32_16x16x32_bf16 v[52:55], v[220:223], v[196:199], v[52:55]\n"
      "v_mfma_f32_16x16x32_bf16 v[20:23], v[220:223], v[192:195], v[20:23]\n"
      "s_waitcnt lgkmcnt(0)\n"
      "v_mfma_f32_16x16x32_bf16 v[24:27], v[224:227], v[192:195], v[24:27]\n"
      "v_mfma_f32_16x16x32_bf16 v[56:59], v[224:227], v[196:199], v[56:59]\n"
      "v_mfma_f32_16x16x32_bf16 v[88:91], v[224:227], v[200:203], v[88:91]\n"
      "v_mfma_f32_16x16x32_bf16 v[92:95], v[228:231], v[200:203], v[92:95]\n"
      "v_mfma_f32_16x16x32_bf16 v[60:63], v[228:231], v[196:199], v[60:63]\n"
      "v_mfma_f32_16x16x32_bf16 v[28:31], v[228:231], v[192:195], v[28:31]\n"
      "v_mul_f32_e32 v0, v235, v0\n"
      "v_mul_f32_e32 v1, v235, v1\n"
      "v_mul_f32_e32 v2, v235, v2\n"
      "v_mul_f32_e32 v3, v235, v3\n"
      "v_cvt_pk_bf16_f32 v0, v0, v1\n"
      "v_cvt_pk_bf16_f32 v1, v2, v3\n"
      "v_mul_f32_e32 v4, v235, v4\n"
      "v_mul_f32_e32 v5, v235, v5\n"
      "v_mul_f32_e32 v6, v235, v6\n"
      "v_mul_f32_e32 v7, v235, v7\n"
      "v_cvt_pk_bf16_f32 v4, v4, v5\n"
      "v_cvt_pk_bf16_f32 v5, v6, v7\n"
      "v_mul_f32_e32 v8, v235, v8\n"
      "v_mul_f32_e32 v9, v235, v9\n"
      "v_mul_f32_e32 v10, v235, v10\n"
      "v_mul_f32_e32 v11, v235, v11\n"
      "v_cvt_pk_bf16_f32 v8, v8, v9\n"
      "v_cvt_pk_bf16_f32 v9, v10, v11\n"
      "ds_write_b64 %5, v[0:1] offset:0\n"
      "v_mul_f32_e32 v12, v235, v12\n"
      "v_mul_f32_e32 v13, v235, v13\n"
      "v_mul_f32_e32 v14, v235, v14\n"
      "v_mul_f32_e32 v15, v235, v15\n"
      "v_cvt_pk_bf16_f32 v12, v12, v13\n"
      "v_cvt_pk_bf16_f32 v13, v14, v15\n"
      "ds_write_b64 v238, v[4:5] offset:0\n"
      "v_mul_f32_e32 v16, v235, v16\n"
      "v_mul_f32_e32 v17, v235, v17\n"
      "v_mul_f32_e32 v18, v235, v18\n"
      "v_mul_f32_e32 v19, v235, v19\n"
      "v_cvt_pk_bf16_f32 v16, v16, v17\n"
      "v_cvt_pk_bf16_f32 v17, v18, v19\n"
      "ds_write_b64 v239, v[8:9] offset:0\n"
      "v_mul_f32_e32 v20, v235, v20\n"
      "v_mul_f32_e32 v21, v235, v21\n"
      "v_mul_f32_e32 v22, v235, v22\n"
      "v_mul_f32_e32 v23, v235, v23\n"
      "v_cvt_pk_bf16_f32 v20, v20, v21\n"
      "v_cvt_pk_bf16_f32 v21, v22, v23\n"
      "ds_write_b64 v240, v[12:13] offset:0\n"
      "v_mul_f32_e32 v24, v235, v24\n"
      "v_mul_f32_e32 v25, v235, v25\n"
      "v_mul_f32_e32 v26, v235, v26\n"
      "v_mul_f32_e32 v27, v235, v27\n"
      "v_cvt_pk_bf16_f32 v24, v24, v25\n"
      "v_cvt_pk_bf16_f32 v25, v26, v27\n"
      "ds_write_b64 %5, v[16:17] offset:128\n"
      "v_mul_f32_e32 v28, v235, v28\n"
      "v_mul_f32_e32 v29, v235, v29\n"
      "v_mul_f32_e32 v30, v235, v30\n"
      "v_mul_f32_e32 v31, v235, v31\n"
      "v_cvt_pk_bf16_f32 v28, v28, v29\n"
      "v_cvt_pk_bf16_f32 v29, v30, v31\n"
      "ds_write_b64 v238, v[20:21] offset:128\n"
      "ds_write_b64 v239, v[24:25] offset:128\n"
      "ds_write_b64 v240, v[28:29] offset:128\n"
      "s_waitcnt lgkmcnt(0)\n"
      "s_barrier\n"
      "v_mul_f32_e32 v32, v236, v32\n"
      "v_mul_f32_e32 v33, v236, v33\n"
      "v_mul_f32_e32 v34, v236, v34\n"
      "v_mul_f32_e32 v35, v236, v35\n"
      "v_cvt_pk_bf16_f32 v32, v32, v33\n"
      "v_cvt_pk_bf16_f32 v33, v34, v35\n"
      "v_mul_f32_e32 v36, v236, v36\n"
      "v_mul_f32_e32 v37, v236, v37\n"
      "v_mul_f32_e32 v38, v236, v38\n"
      "v_mul_f32_e32 v39, v236, v39\n"
      "v_cvt_pk_bf16_f32 v36, v36, v37\n"
      "v_cvt_pk_bf16_f32 v37, v38, v39\n"
      "v_mul_f32_e32 v40, v236, v40\n"
      "v_mul_f32_e32 v41, v236, v41\n"
      "v_mul_f32_e32 v42, v236, v42\n"
      "v_mul_f32_e32 v43, v236, v43\n"
      "v_cvt_pk_bf16_f32 v40, v40, v41\n"
      "v_cvt_pk_bf16_f32 v41, v42, v43\n"
      "ds_write_b64 %5, v[32:33] offset:4096\n"
      "v_mul_f32_e32 v44, v236, v44\n"
      "v_mul_f32_e32 v45, v236, v45\n"
      "v_mul_f32_e32 v46, v236, v46\n"
      "v_mul_f32_e32 v47, v236, v47\n"
      "v_cvt_pk_bf16_f32 v44, v44, v45\n"
      "v_cvt_pk_bf16_f32 v45, v46, v47\n"
      "ds_write_b64 v238, v[36:37] offset:4096\n"
      "v_mul_f32_e32 v48, v236, v48\n"
      "v_mul_f32_e32 v49, v236, v49\n"
      "v_mul_f32_e32 v50, v236, v50\n"
      "v_mul_f32_e32 v51, v236, v51\n"
      "v_cvt_pk_bf16_f32 v48, v48, v49\n"
      "v_cvt_pk_bf16_f32 v49, v50, v51\n"
      "ds_write_b64 v239, v[40:41] offset:4096\n"
      "v_mul_f32_e32 v52, v236, v52\n"
      "v_mul_f32_e32 v53, v236, v53\n"
      "v_mul_f32_e32 v54, v236, v54\n"
      "v_mul_f32_e32 v55, v236, v55\n"
      "v_cvt_pk_bf16_f32 v52, v52, v53\n"
      "v_cvt_pk_bf16_f32 v53, v54, v55\n"
      "ds_write_b64 v240, v[44:45] offset:4096\n"
      "v_mul_f32_e32 v56, v236, v56\n"
      "v_mul_f32_e32 v57, v236, v57\n"
      "v_mul_f32_e32 v58, v236, v58\n"
      "v_mul_f32_e32 v59, v236, v59\n"
      "v_cvt_pk_bf16_f32 v56, v56, v57\n"
      "v_cvt_pk_bf16_f32 v57, v58, v59\n"
      "ds_write_b64 %5, v[48:49] offset:4224\n"
      "v_mul_f32_e32 v60, v236, v60\n"
      "v_mul_f32_e32 v61, v236, v61\n"
      "v_mul_f32_e32 v62, v236, v62\n"
      "v_mul_f32_e32 v63, v236, v63\n"
      "v_cvt_pk_bf16_f32 v60, v60, v61\n"
      "v_cvt_pk_bf16_f32 v61, v62, v63\n"
      "ds_write_b64 v238, v[52:53] offset:4224\n"
      "ds_write_b64 v239, v[56:57] offset:4224\n"
      "ds_write_b64 v240, v[60:61] offset:4224\n"
      "s_waitcnt lgkmcnt(0)\n"
      "s_barrier\n"
      "v_mul_f32_e32 v64, v237, v64\n"
      "v_mul_f32_e32 v65, v237, v65\n"
      "v_mul_f32_e32 v66, v237, v66\n"
      "v_mul_f32_e32 v67, v237, v67\n"
      "v_cvt_pk_bf16_f32 v64, v64, v65\n"
      "v_cvt_pk_bf16_f32 v65, v66, v67\n"
      "v_mul_f32_e32 v68, v237, v68\n"
      "v_mul_f32_e32 v69, v237, v69\n"
      "v_mul_f32_e32 v70, v237, v70\n"
      "v_mul_f32_e32 v71, v237, v71\n"
      "v_cvt_pk_bf16_f32 v68, v68, v69\n"
      "v_cvt_pk_bf16_f32 v69, v70, v71\n"
      "v_mul_f32_e32 v72, v237, v72\n"
      "v_mul_f32_e32 v73, v237, v73\n"
      "v_mul_f32_e32 v74, v237, v74\n"
      "v_mul_f32_e32 v75, v237, v75\n"
      "v_cvt_pk_bf16_f32 v72, v72, v73\n"
      "v_cvt_pk_bf16_f32 v73, v74, v75\n"
      "ds_write_b64 %5, v[64:65] offset:8192\n"
      "v_mul_f32_e32 v76, v237, v76\n"
      "v_mul_f32_e32 v77, v237, v77\n"
      "v_mul_f32_e32 v78, v237, v78\n"
      "v_mul_f32_e32 v79, v237, v79\n"
      "v_cvt_pk_bf16_f32 v76, v76, v77\n"
      "v_cvt_pk_bf16_f32 v77, v78, v79\n"
      "ds_write_b64 v238, v[68:69] offset:8192\n"
      "v_mul_f32_e32 v80, v237, v80\n"
      "v_mul_f32_e32 v81, v237, v81\n"
      "v_mul_f32_e32 v82, v237, v82\n"
      "v_mul_f32_e32 v83, v237, v83\n"
      "v_cvt_pk_bf16_f32 v80, v80, v81\n"
      "v_cvt_pk_bf16_f32 v81, v82, v83\n"
      "ds_write_b64 v239, v[72:73] offset:8192\n"
      "v_mul_f32_e32 v84, v237, v84\n"
      "v_mul_f32_e32 v85, v237, v85\n"
      "v_mul_f32_e32 v86, v237, v86\n"
      "v_mul_f32_e32 v87, v237, v87\n"
      "v_cvt_pk_bf16_f32 v84, v84, v85\n"
      "v_cvt_pk_bf16_f32 v85, v86, v87\n"
      "ds_write_b64 v240, v[76:77] offset:8192\n"
      "v_mul_f32_e32 v88, v237, v88\n"
      "v_mul_f32_e32 v89, v237, v89\n"
      "v_mul_f32_e32 v90, v237, v90\n"
      "v_mul_f32_e32 v91, v237, v91\n"
      "v_cvt_pk_bf16_f32 v88, v88, v89\n"
      "v_cvt_pk_bf16_f32 v89, v90, v91\n"
      "ds_write_b64 %5, v[80:81] offset:8320\n"
      "v_mul_f32_e32 v92, v237, v92\n"
      "v_mul_f32_e32 v93, v237, v93\n"
      "v_mul_f32_e32 v94, v237, v94\n"
      "v_mul_f32_e32 v95, v237, v95\n"
      "v_cvt_pk_bf16_f32 v92, v92, v93\n"
      "v_cvt_pk_bf16_f32 v93, v94, v95\n"
      "ds_write_b64 v238, v[84:85] offset:8320\n"
      "ds_write_b64 v239, v[88:89] offset:8320\n"
      "ds_write_b64 v240, v[92:93] offset:8320\n"
      "s_waitcnt lgkmcnt(0)\n"
      "s_barrier\n"
      "s_waitcnt lgkmcnt(0)\n"
      "s_barrier\n"
      "CEND%=:\n"
      : "=&s"(flag)
      : "v"(ldsb + f_kbase(lane)), "v"(vb), "v"(vb ^ 32u),
        "v"(ldsb + QST + w * 12288u + (i16 >> 2) * 1024u + (i16 & 3u) * 64u + ((g ^ sig(i16 >> 2)) << 4)),
        "v"(0 ? ldsb + OST + w * 12288u + lane * 8u : ldsb + OST + w * 12288u + i16 * 256u + (((g >> 1) ^ (i16 & 7u)) << 4) + 8u * (g & 1u)),
        "v"(h0 * QBYTES + w * 12288u + i16 * 256u + g * 16u), "s"((unsigned long long)Q & 0xffffffffffffull), "s"(w * 8192u)
      : "memory", "scc", "vcc", "v0", "v1", "v2", "v3", "v4", "v5", "v6", "v7", "v8", "v9", "v10", "v11", "v12", "v13", "v14", "v15", "v16", "v17", "v18", "v19", "v20", "v21", "v22", "v23", "v24", "v25", "v26", "v27", "v28", "v29", "v30", "v31", "v32", "v33", "v34", "v35", "v36", "v37", "v38", "v39", "v40", "v41", "v42", "v43", "v44", "v45", "v46", "v47", "v48", "v49", "v50", "v51", "v52", "v53", "v54", "v55", "v56", "v57", "v58", "v59", "v60", "v61", "v62", "v63", "v64", "v65", "v66", "v67", "v68", "v69", "v70", "v71", "v72", "v73", "v74", "v75", "v76", "v77", "v78", "v79", "v80", "v81", "v82", "v83", "v84", "v85", "v86", "v87", "v88", "v89", "v90", "v91", "v92", "v93", "v94", "v95", "v96", "v97", "v98", "v99", "v100", "v101", "v102", "v103", "v104", "v105", "v106", "v107", "v108", "v109", "v110", "v111", "v112", "v113", "v114", "v115", "v116", "v117", "v118", "v119", "v120", "v121", "v122", "v123", "v124", "v125", "v126", "v127", "v128", "v129", "v130", "v131", "v132", "v133", "v134", "v135", "v136", "v137", "v138", "v139", "v140", "v141", "v142", "v143", "v144", "v145", "v146", "v147", "v148", "v149", "v150", "v151", "v152", "v153", "v154", "v155", "v156", "v157", "v158", "v159", "v160", "v161", "v162", "v163", "v164", "v165", "v166", "v167", "v168", "v169", "v170", "v171", "v172", "v173", "v174", "v175", "v176", "v177", "v178", "v179", "v180", "v181", "v182", "v183", "v184", "v185", "v186", "v187", "v188", "v189", "v190", "v191", "v192", "v193", "v194", "v195", "v196", "v197", "v198", "v199", "v200", "v201", "v202", "v203", "v204", "v205", "v206", "v207", "v208", "v209", "v210", "v211", "v212", "v213", "v214", "v215", "v216", "v217", "v218", "v219", "v220", "v221", "v222", "v223", "v224", "v225", "v226", "v227", "v228", "v229", "v230", "v231", "v232", "v233", "v234", "v235", "v236", "v237", "v238", "v239", "v240", "s40", "s41", "s42", "s43", "s44", "s45", "s46", "s47", "s48", "s49", "s50", "s51", "s52", "s53", "s54", "s55", "s56", "s57");
  }
  unsigned lane;
  asm volatile("v_mbcnt_lo_u32_b32 %0, -1, 0\n\tv_mbcnt_hi_u32_b32 %0, -1, %0" : "=v"(lane) :: "memory");
  volatile LAS unsigned* vote = (volatile LAS unsigned*)lds;
  vote[w] = flag;
  asm volatile("s_waitcnt lgkmcnt(0)\n\ts_barrier" ::: "memory");
  const unsigned redo = __builtin_amdgcn_readfirstlane(vote[0] | vote[1] | vote[2] | vote[3]);
  if (__builtin_expect(redo != 0 && !(KDIAG && SAMEHEAD), 0)) {
#pragma nounroll
    for (int hi = 0; hi < HPW; ++hi)
      if ((redo >> hi) & 1u) safe_head(Q, K, V, O, h0 + hi * hs, w, lane, ldsb);
  }
}

extern "C" void kernel_launch(void* const* d_in, const int* in_sizes, int n_in,
                              void* d_out, int out_size, void* d_ws, size_t ws_size,
                              hipStream_t stream) {
  (void)in_sizes; (void)n_in; (void)out_size; (void)d_ws; (void)ws_size;
  (void)hipFuncSetAttribute((const void*)attn_fwd, hipFuncAttributeMaxDynamicSharedMemorySize, LDS_BYTES);
  hipLaunchKernelGGL(attn_fwd, dim3(NHEAD / HPW), dim3(512), LDS_BYTES, stream,
                     (const __bf16*)d_in[0], (const __bf16*)d_in[1], (const __bf16*)d_in[2], (__bf16*)d_out
                     );
}
```
